```python
import torch

def _softmax(x, axis=-1):
    return torch.softmax(x, dim=axis)
def _gelu(x):
    return torch.nn.functional.gelu(x, approximate="tanh")
def _silu(x):
    return torch.nn.functional.silu(x)
def _sigmoid(x):
    return torch.sigmoid(x)
def _layernorm(x, axis=-1, eps=1e-5):
    m = x.mean(dim=axis, keepdim=True)
    v = x.var(dim=axis, keepdim=True, unbiased=False)
    return (x - m) / torch.sqrt(v + eps)
def _scatter_add(tgt, idx, src, axis=0):
    return tgt.clone().index_add_(axis, idx, src)
def _segment_sum(src, seg, n):
    out = torch.zeros((n,) + src.shape[1:], dtype=src.dtype)
    return out.index_add_(0, seg, src)
def _pad(x, pw):
    flat = tuple(v for pair in reversed(pw) for v in pair)
    return torch.nn.functional.pad(x, flat)

kernel_name = 'sequential_preLN__spectral_gfnet__glu_gated_glu__prompt'

def setup_inputs():

    _w = (lambda a: (lambda t: t.to(torch.bfloat16) if t.is_floating_point() else t)(torch.as_tensor(a)))
    def _A_setup():
        B, S, D = 1, 2048, 512
        Df = D // 2 + 1
        return {
            "x": _w((lambda *s: torch.randn(*s, dtype=torch.bfloat16) * 0.05)(B, S, D)),
            "w_r": _w((lambda *s: torch.randn(*s, dtype=torch.bfloat16) * 0.4)(S, Df) * 0.02),
            "w_i": _w((lambda *s: torch.randn(*s, dtype=torch.bfloat16) * 0.4)(S, Df) * 0.02),
        }
    _A_in = _A_setup()

    def _B_setup():
        B, S, D, F = 1, 2048, 512, 1792
        return {
            "x": _w((lambda *s: torch.randn(*s, dtype=torch.bfloat16) * 0.05)(B, S, D)),
            "w_gate": _w((lambda *s: torch.randn(*s, dtype=torch.bfloat16) * 0.05)(D, F)),
            "w_up": _w((lambda *s: torch.randn(*s, dtype=torch.bfloat16) * 0.05)(D, F)),
            "w_down": _w((lambda *s: torch.randn(*s, dtype=torch.bfloat16) * 0.05)(F, D)),
        }
    _B_in = _B_setup()

    return {
        "A_x": _A_in["x"],
        "A_w_r": _A_in["w_r"],
        "A_w_i": _A_in["w_i"],
        "B_x": _B_in["x"],
        "B_w_gate": _B_in["w_gate"],
        "B_w_up": _B_in["w_up"],
        "B_w_down": _B_in["w_down"],
    }

def reference(A_x, A_w_r, A_w_i, B_x, B_w_gate, B_w_up, B_w_down):
    def _A_ref(x, w_r, w_i):
        B, S, D = x.shape
        X = (lambda x, axes=(-2,-1): torch.fft.rfft2(x.float(), dim=axes))(x, axes=(-2, -1))
        W = w_r + 1j * w_i
        X = X * W
        y = (lambda x, s, axes=(-2,-1): torch.fft.irfft2(x, s=s, dim=axes).to(torch.bfloat16))(X, (S, D), axes=(-2, -1))
        return y

    def _B_ref(x, w_gate, w_up, w_down):
        g = torch.einsum("bsd,df->bsf", x, w_gate)
        u = torch.einsum("bsd,df->bsf", x, w_up)
        h = _sigmoid(g) * u
        return torch.einsum("bsf,fd->bsd", h, w_down)

    x = A_x
    h = x + _A_ref(_layernorm(x, axis=-1), A_w_r, A_w_i)
    h2 = h + _B_ref(_layernorm(h, axis=-1), B_w_gate, B_w_up, B_w_down)
    return h2
```

```cpp
#include <hip/hip_runtime.h>
#include <stdint.h>

namespace {
typedef unsigned u32;
typedef unsigned long long u64;
typedef unsigned short u16;
typedef __attribute__((ext_vector_type(2))) __bf16 bf2;
typedef __attribute__((ext_vector_type(8))) __bf16 bf8;
typedef __attribute__((ext_vector_type(2))) _Float16 hf2;
typedef __attribute__((ext_vector_type(8))) _Float16 hf8;
typedef __attribute__((ext_vector_type(2))) float f2;
typedef __attribute__((ext_vector_type(4))) float f4;
typedef __attribute__((ext_vector_type(16))) float f16v;
typedef __attribute__((ext_vector_type(2))) unsigned u2;
typedef __attribute__((ext_vector_type(4))) unsigned u4;
#define DI __device__ __forceinline__
#define LAS __attribute__((address_space(3)))
typedef __amdgpu_buffer_rsrc_t rsrc_t;

constexpr int kS = 2048, kD = 512, kF = 1792;
constexpr u32 XW_OFF = 0;
constexpr u32 HH_OFF = 8u << 20;
constexpr u32 YH_OFF = 10u << 20;
constexpr u32 WP_OFF = 12u << 20;
constexpr u32 HP_OFF = 16u << 20;
constexpr u32 CT_OFF = 24u << 20;
constexpr u32 WS_BYTES = 25u << 20;
constexpr float kEps = 1e-5f;

constexpr int L_P1IMG = 0, L_STD = 16384, L_P0IMG = 20480, L_XIMG = 40960, L_WIMG = 49152, L_SIMG = 57344, L_EXCH = 61440;
constexpr int L_BYTES = 114688;

DI u32 pk_bf(float a, float b) { f2 v = {a, b}; return __builtin_bit_cast(u32, __builtin_convertvector(v, bf2)); }
DI u32 pk_hf(float a, float b) { f2 v = {a, b}; return __builtin_bit_cast(u32, __builtin_convertvector(v, hf2)); }
template <bool BF> DI u32 pk(float a, float b) { return BF ? pk_bf(a, b) : pk_hf(a, b); }
DI float bf_lo(u32 v) { return __builtin_bit_cast(float, v << 16); }
DI float bf_hi(u32 v) { return __builtin_bit_cast(float, v & 0xffff0000u); }
DI float hf_lo(u32 v) { return (float)__builtin_bit_cast(hf2, v)[0]; }
DI float hf_hi(u32 v) { return (float)__builtin_bit_cast(hf2, v)[1]; }
DI float dot2sq(u32 v, float c) { const bf2 x = __builtin_bit_cast(bf2, v); return __builtin_amdgcn_fdot2_f32_bf16(x, x, c, false); }
DI float cosr(float r) { return __builtin_amdgcn_cosf(r); }
DI float sinr(float r) { return __builtin_amdgcn_sinf(r); }
DI f4 mm16b(u4 a, u4 b, f4 c) { return __builtin_amdgcn_mfma_f32_16x16x32_bf16(__builtin_bit_cast(bf8, a), __builtin_bit_cast(bf8, b), c, 0, 0, 0); }
DI f4 mm16h(u4 a, u4 b, f4 c) { return __builtin_amdgcn_mfma_f32_16x16x32_f16(__builtin_bit_cast(hf8, a), __builtin_bit_cast(hf8, b), c, 0, 0, 0); }
template <bool BF> DI f16v mm32(u4 a, u4 b, f16v c) {
  if (BF) return __builtin_amdgcn_mfma_f32_32x32x16_bf16(__builtin_bit_cast(bf8, a), __builtin_bit_cast(bf8, b), c, 0, 0, 0);
  return __builtin_amdgcn_mfma_f32_32x32x16_f16(__builtin_bit_cast(hf8, a), __builtin_bit_cast(hf8, b), c, 0, 0, 0);
}
template <int CTRL> DI float dpp_add(float v) {
  return v + __builtin_bit_cast(float, __builtin_amdgcn_update_dpp(0, __builtin_bit_cast(int, v), CTRL, 0xf, 0xf, true));
}
DI float swap32_add(float v) {
  u32 a = __builtin_bit_cast(u32, v), b = a;
  asm("" : "+v"(b));
  auto r = __builtin_amdgcn_permlane32_swap(a, b, false, false);
  const u32 r0 = r[0], r1 = r[1];
  return __builtin_bit_cast(float, r0) + __builtin_bit_cast(float, r1);
}
DI float swap16_add(float v) {
  u32 a = __builtin_bit_cast(u32, v), b = a;
  asm("" : "+v"(b));
  auto r = __builtin_amdgcn_permlane16_swap(a, b, false, false);
  const u32 r0 = r[0], r1 = r[1];
  return __builtin_bit_cast(float, r0) + __builtin_bit_cast(float, r1);
}
DI float red_row_half(float v) {
  v = dpp_add<0x128>(v); v = dpp_add<0x124>(v); v = dpp_add<0x122>(v); v = dpp_add<0x121>(v);
  return swap32_add(v);
}
template <int AUX> DI u4 ld128(rsrc_t r, u32 voff, u32 soff) { return __builtin_amdgcn_raw_buffer_load_b128(r, voff, soff, AUX); }
template <int AUX> DI void st128(u4 v, rsrc_t r, u32 voff, u32 soff) { __builtin_amdgcn_raw_buffer_store_b128(v, r, voff, soff, AUX); }
DI rsrc_t mk_rsrc(const void* p, u32 bytes) { return __builtin_amdgcn_make_buffer_rsrc((void*)p, 0, bytes, 0x00020000); }
DI u4 lds128(LAS char* L, u32 off) { return *(LAS u4*)(L + off); }

struct Args {
  const u16 *x, *wr, *wi, *wg, *wu, *wd;
  u16* out;
  unsigned char* ws;
};

template <bool BF> struct R512 { u4 Bc[2], Bs[2], Are, Aim; };
struct R512T { float c[8], s[8]; };
template <bool BF> DI void r512_build(R512<BF>& k, const float (&c)[16], const float (&s)[16], const float (&ar)[8], const float (&ai)[8]) {
#pragma unroll
  for (int s2 = 0; s2 < 2; ++s2)
#pragma unroll
    for (int p = 0; p < 4; ++p) {
      k.Bc[s2][p] = pk<BF>(c[8 * s2 + 2 * p], c[8 * s2 + 2 * p + 1]);
      k.Bs[s2][p] = pk<BF>(s[8 * s2 + 2 * p], s[8 * s2 + 2 * p + 1]);
    }
#pragma unroll
  for (int p = 0; p < 4; ++p) { k.Are[p] = pk<BF>(ar[2 * p], ar[2 * p + 1]); k.Aim[p] = pk<BF>(ai[2 * p], ai[2 * p + 1]); }
}
DI void r512_consts(int lane, R512<false>& kh, R512<true>& kb, R512T& tw) {
  const int r = lane & 31, h = lane >> 5;
  float c[16], s[16], ar[8], ai[8];
#pragma unroll
  for (int i = 0; i < 16; ++i) {
    const int d0 = 16 * (i >> 3) + 8 * ((i >> 2) & 1) + 4 * h + (i & 3);
    const float rev = (float)((d0 * r) & 31) * (1.0f / 32.0f);
    c[i] = cosr(rev); s[i] = -sinr(rev);
  }
#pragma unroll
  for (int j = 0; j < 8; ++j) {
    const int d1 = (j & 3) + 8 * (j >> 2) + 4 * h;
    const float rev = (float)((d1 * (r & 7)) & 15) * (1.0f / 16.0f);
    const float cc = cosr(rev), ss = sinr(rev);
    const float ny = (j & 1) ? -0.70710678118654752f : 0.70710678118654752f;
    ar[j] = r < 8 ? cc : (r < 16 ? -ss : (r == 16 ? ny : 0.f));
    ai[j] = r < 8 ? ss : (r < 16 ? cc : 0.f);
    const float rt = (float)(d1 * r) * (1.0f / 512.0f);
    tw.c[j] = cosr(rt) * 0.0625f; tw.s[j] = sinr(rt) * 0.0625f;
  }
  r512_build<false>(kh, c, s, ar, ai);
  r512_build<true>(kb, c, s, ar, ai);
}
template <bool BF> DI void r512_run(const R512<BF>& k, const R512T& tw, const u4 (&a)[2], int lane, float (&re)[2][4], float (&im)[2][4]) {
  f16v z;
#pragma unroll
  for (int i = 0; i < 16; ++i) z[i] = 0.f;
  f16v tr = mm32<BF>(a[0], k.Bc[0], z), ti = mm32<BF>(a[0], k.Bs[0], z);
  tr = mm32<BF>(a[1], k.Bc[1], tr); ti = mm32<BF>(a[1], k.Bs[1], ti);
#pragma unroll
  for (int tk = 0; tk < 2; ++tk) {
    u4 br, bi;
#pragma unroll
    for (int p = 0; p < 4; ++p) {
      const int j0 = 2 * p, j1 = 2 * p + 1;
      const float r0 = tr[8 * tk + j0], i0 = ti[8 * tk + j0], r1 = tr[8 * tk + j1], i1 = ti[8 * tk + j1];
      br[p] = pk<BF>(r0 * tw.c[j0] + i0 * tw.s[j0], r1 * tw.c[j1] + i1 * tw.s[j1]);
      bi[p] = pk<BF>(i0 * tw.c[j0] - r0 * tw.s[j0], i1 * tw.c[j1] - r1 * tw.s[j1]);
    }
    f16v o = mm32<BF>(k.Are, br, z);
    o = mm32<BF>(k.Aim, bi, o);
#pragma unroll
    for (int i = 0; i < 4; ++i) { re[tk][i] = o[i]; im[tk][i] = o[4 + i]; }
    if (lane == 0) { re[tk][0] = o[8]; im[tk][0] = 0.f; }
  }
}

template <int AUX> DI void phase1(const Args& p, LAS char* L, rsrc_t ws, int b, int t, const R512<false>& kh, const R512T& tw, u32 tagA, u32 tagB) {
  const int lane = t & 63, w = __builtin_amdgcn_readfirstlane(t >> 6), r = lane & 31, h = lane >> 5, tok = r >> 4, d1 = r & 15;
  const int a = b & 7, n2 = b >> 3;
  const u16* xr = p.x + (size_t)(32 * (8 * a + 2 * w + tok) + n2) * kD + d1 + 64 * h;
  float v[16];
#pragma unroll
  for (int i = 0; i < 16; ++i) v[i] = bf_lo(xr[16 * (16 * (i >> 3) + 8 * ((i >> 2) & 1) + (i & 3))]);
  u32 wq[8];
  {
    const int k1 = 16 * (b >> 6) + (b & 15), g = (b >> 4) & 3, kk = t == 0 ? 256 : t;
#pragma unroll
    for (int e = 0; e < 8; ++e) {
      const int ks = k1 + 64 * (16 * (e >> 2) + 4 * g + (e & 3));
      wq[e] = (u32)p.wr[ks * 257 + kk] | ((u32)p.wi[ks * 257 + kk] << 16);
    }
  }
  float sum = 0.f;
#pragma unroll
  for (int i = 0; i < 16; ++i) sum += v[i];
  const float mean = red_row_half(sum) * (1.0f / 512.0f);
  float sq = 0.f;
#pragma unroll
  for (int i = 0; i < 16; ++i) { v[i] -= mean; sq += v[i] * v[i]; }
  const float var = red_row_half(sq) * (1.0f / 512.0f) + kEps;
  const float rstd = __builtin_amdgcn_rsqf(var);
  u4 af[2];
#pragma unroll
  for (int s2 = 0; s2 < 2; ++s2)
#pragma unroll
    for (int q = 0; q < 4; ++q) af[s2][q] = pk_hf(v[8 * s2 + 2 * q] * rstd, v[8 * s2 + 2 * q + 1] * rstd);
  if (d1 == 0 && h == 0) *(LAS u32*)(L + L_STD + (2 * w + tok) * 4) = pk_bf(var * rstd, 0.f) & 0xffffu;
  float re[2][4], im[2][4];
  r512_run<false>(kh, tw, af, lane, re, im);
#pragma unroll
  for (int i = 0; i < 4; ++i) {
    const int kap = r + 32 * (4 * h + i);
    u2 pl = {pk_hf(re[0][i], re[1][i]), pk_hf(im[0][i], im[1][i])};
    *(LAS u2*)(L + L_P1IMG + kap * 64 + ((w ^ ((kap >> 1) & 7)) * 8)) = pl;
  }
#pragma unroll
  for (int c = 0; c < 4; ++c) {
    u2 pl = {wq[2 * c], wq[2 * c + 1]};
    *(LAS u2*)(L + L_P1IMG + t * 64 + (((4 + c) ^ ((t >> 1) & 7)) * 8)) = pl;
  }
  __syncthreads();
  const int c = t & 7;
  const u32 mid = *(LAS u32*)(L + L_STD + c * 4) | (tagA << 16);
#pragma unroll
  for (int i = 0; i < 8; ++i) {
    const int kap = 32 * i + (t >> 3);
    const u2 pl = *(LAS u2*)(L + L_P1IMG + kap * 64 + ((c ^ ((kap >> 1) & 7)) * 8));
    u4 o = {pl[0], mid, pl[1], tagB};
    st128<AUX>(o, ws, XW_OFF + b * 32768 + t * 16, i * 4096);
  }
}

template <int AUX> DI void phase0(const Args& p, LAS char* L, rsrc_t ws, int b, int t, const R512<true>& kb, const R512T& tw) {
  const int idx = b >> 3;
  if (idx >= 28) return;
  const int tau = 28 * (b & 7) + idx;
  const int lane = t & 63, w = __builtin_amdgcn_readfirstlane(t >> 6), r = lane & 31, h = lane >> 5, cb = r >> 4, d1 = r & 15;
  const u16* src = (cb ? p.wu : p.wg) + (size_t)(d1 + 64 * h) * kF + 8 * tau + 2 * w;
  u32 v[16];
#pragma unroll
  for (int i = 0; i < 16; ++i) v[i] = *(const u32*)(src + (size_t)(16 * (16 * (i >> 3) + 8 * ((i >> 2) & 1) + (i & 3))) * kF);
#pragma unroll
  for (int ps = 0; ps < 2; ++ps) {
    u4 af[2];
#pragma unroll
    for (int s2 = 0; s2 < 2; ++s2)
#pragma unroll
      for (int q = 0; q < 4; ++q)
        af[s2][q] = __builtin_amdgcn_perm(v[8 * s2 + 2 * q + 1], v[8 * s2 + 2 * q], ps ? 0x07060302u : 0x05040100u);
    float re[2][4], im[2][4];
    r512_run<true>(kb, tw, af, lane, re, im);
#pragma unroll
    for (int tk = 0; tk < 2; ++tk) {
      const int m = 4 * w + 2 * tk + ps, q = (r >> 2) + 8 * h, g = r & 3;
      u4 o;
#pragma unroll
      for (int i = 0; i < 4; ++i) o[i] = pk_bf(re[tk][i], im[tk][i]);
      *(LAS u4*)(L + L_P0IMG + q * 1024 + g * 256 + ((m ^ (r & 7)) * 16)) = o;
    }
  }
  __syncthreads();
  const int fb = tau / 7, mt = tau - 7 * fb;
#pragma unroll
  for (int i = 0; i < 4; ++i) {
    const int q = 4 * i + w, g = lane >> 4, m = lane & 15;
    const u4 o = lds128(L, L_P0IMG + q * 1024 + g * 256 + ((m ^ (((q & 1) << 2) | g)) * 16));
    st128<AUX>(o, ws, WP_OFF + ((fb * 16 + q) * 7 + mt) * 1024 + lane * 16, 0);
  }
}

struct P2K { u4 F1re[4], F1im[4], G1re[4], G1im[4], C[2], S[2], nS[2]; float fc[8], fs[8], ic[8], is[8]; };
DI void p2_consts(int lane, int w, P2K& k) {
  const int row = lane & 15, g = lane >> 4, id = 16 * w + row;
#pragma unroll
  for (int q = 0; q < 4; ++q) {
    float c[4], s[4];
#pragma unroll
    for (int e = 0; e < 4; ++e) {
      const float rev = (float)((id * (16 * q + 4 * g + e)) & 63) * (1.0f / 64.0f);
      c[e] = cosr(rev); s[e] = sinr(rev);
    }
    k.F1re[q] = u4{pk_hf(c[0], c[1]), pk_hf(s[0], s[1]), pk_hf(c[2], c[3]), pk_hf(s[2], s[3])};
    k.F1im[q] = u4{pk_hf(-s[0], -s[1]), pk_hf(c[0], c[1]), pk_hf(-s[2], -s[3]), pk_hf(c[2], c[3])};
    k.G1re[q] = u4{pk_bf(c[0], c[1]), pk_bf(s[0], s[1]), pk_bf(c[2], c[3]), pk_bf(s[2], s[3])};
    k.G1im[q] = u4{pk_bf(-s[0], -s[1]), pk_bf(c[0], c[1]), pk_bf(-s[2], -s[3]), pk_bf(c[2], c[3])};
  }
#pragma unroll
  for (int tt = 0; tt < 2; ++tt) {
    float c[8], s[8];
#pragma unroll
    for (int e = 0; e < 8; ++e) {
      const float rev = (float)(((16 * tt + row) * (16 * (e >> 2) + 4 * g + (e & 3))) & 31) * (1.0f / 32.0f);
      c[e] = cosr(rev); s[e] = sinr(rev);
    }
#pragma unroll
    for (int q = 0; q < 4; ++q) {
      k.C[tt][q] = pk_bf(c[2 * q], c[2 * q + 1]); k.S[tt][q] = pk_bf(s[2 * q], s[2 * q + 1]); k.nS[tt][q] = k.S[tt][q] ^ 0x80008000u;
    }
#pragma unroll
    for (int rr = 0; rr < 4; ++rr) {
      const float rf = (float)((16 * tt + 4 * g + rr) * id) * (1.0f / 2048.0f);
      k.fc[4 * tt + rr] = cosr(rf) * (1.0f / 2048.0f); k.fs[4 * tt + rr] = sinr(rf) * (1.0f / 2048.0f);
      const float ri = (float)((16 * tt + row) * (16 * w + 4 * g + rr)) * (1.0f / 2048.0f);
      k.ic[4 * tt + rr] = cosr(ri); k.is[4 * tt + rr] = sinr(ri);
    }
  }
}
template <int AUX> DI void phase2(LAS char* L, rsrc_t ws, int kap, int t, const P2K& k) {
  const int lane = t & 63, w = __builtin_amdgcn_readfirstlane(t >> 6), row = lane & 15, g = lane >> 4;
  u4 ld[8];
#pragma unroll
  for (int i = 0; i < 8; ++i) ld[i] = ld128<AUX>(ws, XW_OFF + (t >> 3) * 32768 + kap * 128 + (t & 7) * 16, i * (32 * 32768));
  {
    const int c = t & 7, sa = (t >> 3) & 7;
    const u32 base = c < 4 ? L_XIMG + ((sa * 2 + (c >> 1)) * 32 + w) * 16 + (c & 1) * 8 : L_WIMG + (t >> 3) * 32 + (c - 4) * 8;
    const u32 step = c < 4 ? 64 : 1024;
#pragma unroll
    for (int i = 0; i < 8; ++i) {
      *(LAS u2*)(L + base + i * step) = u2{ld[i][0], ld[i][2]};
      *(LAS u16*)(L + L_SIMG + (32 * i + (t >> 3)) * 16 + c * 2) = (u16)ld[i][1];
    }
  }
  __syncthreads();
  const f4 z = {0.f, 0.f, 0.f, 0.f};
  f4 tr[2] = {z, z}, ti[2] = {z, z};
#pragma unroll
  for (int q = 0; q < 4; ++q)
#pragma unroll
    for (int tt = 0; tt < 2; ++tt) {
      const u4 a = lds128(L, L_XIMG + ((4 * q + g) * 32 + 16 * tt + row) * 16);
      tr[tt] = mm16h(a, k.F1re[q], tr[tt]); ti[tt] = mm16h(a, k.F1im[q], ti[tt]);
    }
  const u4 w0 = lds128(L, L_WIMG + t * 32), w1 = lds128(L, L_WIMG + t * 32 + 16);
  u4 br, bi;
#pragma unroll
  for (int tt = 0; tt < 2; ++tt)
#pragma unroll
    for (int q = 0; q < 2; ++q) {
      const int e0 = 4 * tt + 2 * q, e1 = e0 + 1;
      const float r0 = tr[tt][2 * q], i0 = ti[tt][2 * q], r1 = tr[tt][2 * q + 1], i1 = ti[tt][2 * q + 1];
      br[2 * tt + q] = pk_bf(r0 * k.fc[e0] + i0 * k.fs[e0], r1 * k.fc[e1] + i1 * k.fs[e1]);
      bi[2 * tt + q] = pk_bf(i0 * k.fc[e0] - r0 * k.fs[e0], i1 * k.fc[e1] - r1 * k.fs[e1]);
    }
  u4 ar, ai;
#pragma unroll
  for (int tt = 0; tt < 2; ++tt) {
    f4 dr = mm16b(k.C[tt], br, z), di = mm16b(k.C[tt], bi, z);
    dr = mm16b(k.S[tt], bi, dr); di = mm16b(k.nS[tt], br, di);
    const u4 wv = tt ? w1 : w0;
#pragma unroll
    for (int q = 0; q < 2; ++q) {
      const float a0 = bf_lo(wv[2 * q]), b0 = bf_hi(wv[2 * q]), a1 = bf_lo(wv[2 * q + 1]), b1 = bf_hi(wv[2 * q + 1]);
      ar[2 * tt + q] = pk_bf(dr[2 * q] * a0 - di[2 * q] * b0, dr[2 * q + 1] * a1 - di[2 * q + 1] * b1);
      ai[2 * tt + q] = pk_bf(dr[2 * q] * b0 + di[2 * q] * a0, dr[2 * q + 1] * b1 + di[2 * q + 1] * a1);
    }
  }
#pragma unroll
  for (int tt = 0; tt < 2; ++tt) {
    f4 dr = mm16b(ar, k.C[tt], z), di = mm16b(ar, k.S[tt], z);
    dr = mm16b(ai, k.nS[tt], dr); di = mm16b(ai, k.C[tt], di);
    float xr[4], xi[4];
#pragma unroll
    for (int rr = 0; rr < 4; ++rr) {
      xr[rr] = dr[rr] * k.ic[4 * tt + rr] - di[rr] * k.is[4 * tt + rr];
      xi[rr] = dr[rr] * k.is[4 * tt + rr] + di[rr] * k.ic[4 * tt + rr];
    }
    *(LAS u4*)(L + L_EXCH + (w * 2 + tt) * 1024 + lane * 16) = u4{pk_bf(xi[0], xi[1]), pk_bf(xr[0], xr[1]), pk_bf(xi[2], xi[3]), pk_bf(xr[2], xr[3])};
  }
  __syncthreads();
#pragma unroll
  for (int tt = 0; tt < 2; ++tt) {
    f4 yr = z, yi = z;
#pragma unroll
    for (int q = 0; q < 4; ++q) {
      const u4 e = lds128(L, L_EXCH + (q * 2 + tt) * 1024 + lane * 16);
      yi = mm16b(k.G1re[q], e, yi); yr = mm16b(k.G1im[q], e, yr);
    }
    const u4 xv = lds128(L, L_XIMG + ((4 * w + g) * 32 + 16 * tt + row) * 16);
    const u2 sd = *(LAS u2*)(L + L_SIMG + ((2 * w + (g >> 1)) + 8 * (16 * tt + row)) * 16 + (g & 1) * 8);
    const float st[4] = {bf_lo(sd[0]), bf_hi(sd[0]), bf_lo(sd[1]), bf_hi(sd[1])};
    const float xre[4] = {hf_lo(xv[0]), hf_hi(xv[0]), hf_lo(xv[2]), hf_hi(xv[2])};
    const float xim[4] = {hf_lo(xv[1]), hf_hi(xv[1]), hf_lo(xv[3]), hf_hi(xv[3])};
    u4 ho, yo;
#pragma unroll
    for (int rr = 0; rr < 4; ++rr) {
      const float yim = kap == 0 ? 0.f : yi[rr];
      ho[rr] = pk_bf(st[rr] * xre[rr] + yr[rr], st[rr] * xim[rr] + yim);
      yo[rr] = pk_bf(yr[rr], yim);
    }
    const u32 off = kap * 8192 + ((4 * w + g) * 32 + 16 * tt + row) * 16;
    st128<AUX>(ho, ws, HH_OFF + off, 0);
    st128<AUX>(yo, ws, YH_OFF + off, 0);
  }
}

template <int AUX> DI void load_wp(LAS char* L, rsrc_t ws, int fb, int t) {
#pragma unroll
  for (int bt = 0; bt < 4; ++bt) {
    u4 v[7];
#pragma unroll
    for (int i = 0; i < 7; ++i) v[i] = ld128<AUX>(ws, WP_OFF + fb * 114688 + t * 16, (bt * 7 + i) * 4096);
#pragma unroll
    for (int i = 0; i < 7; ++i) *(LAS u4*)(L + (bt * 7 + i) * 4096 + t * 16) = v[i];
  }
}
DI void tr4(u32& r0, u32& r1, u32& r2, u32& r3) {
  auto a = __builtin_amdgcn_permlane32_swap(r0, r2, false, false);
  auto b = __builtin_amdgcn_permlane32_swap(r1, r3, false, false);
  auto c = __builtin_amdgcn_permlane16_swap(a[0], b[0], false, false);
  auto d = __builtin_amdgcn_permlane16_swap(a[1], b[1], false, false);
  r0 = c[0]; r1 = c[1]; r2 = d[0]; r3 = d[1];
}
DI u32 kap_off(int q, int i) { return (u32)(4 * (q & 7) + 128 * (q >> 3) + 32 * i) * 8192u; }
template <int AUX> DI void phase4(LAS char* L, rsrc_t ws, int b, int t) {
  const int lane = t & 63, w = __builtin_amdgcn_readfirstlane(t >> 6), c16 = lane & 15, g = lane >> 4;
  const int a = b & 7, fb = b >> 3;
  const u32 voff = HH_OFF + g * 8192 + (a * 64 + 16 * w + c16) * 16;
  constexpr int RING = 8;
  u4 ring[RING][4];
#pragma unroll
  for (int q = 0; q < RING; ++q)
#pragma unroll
    for (int i = 0; i < 4; ++i) ring[q][i] = ld128<AUX>(ws, voff, kap_off(q, i));
  __builtin_amdgcn_sched_barrier(0);
  f4 acc[7][4];
#pragma unroll
  for (int mt = 0; mt < 7; ++mt)
#pragma unroll
    for (int j = 0; j < 4; ++j) acc[mt][j] = f4{0.f, 0.f, 0.f, 0.f};
  float ss[4] = {0.f, 0.f, 0.f, 0.f};
#pragma unroll
  for (int q = 0; q < 16; ++q) {
    u4 bfr[4];
#pragma unroll
    for (int j = 0; j < 4; ++j) bfr[j] = u4{ring[q % RING][0][j], ring[q % RING][1][j], ring[q % RING][2][j], ring[q % RING][3][j]};
    if (q + RING < 16) {
#pragma unroll
      for (int i = 0; i < 4; ++i) ring[q % RING][i] = ld128<AUX>(ws, voff, kap_off(q + RING, i));
    }
#pragma unroll
    for (int j = 0; j < 4; ++j)
#pragma unroll
      for (int i = 0; i < 4; ++i) ss[j] = dot2sq(bfr[j][i], ss[j]);
#pragma unroll
    for (int mt = 0; mt < 7; ++mt) {
      const u4 af = lds128(L, (q * 7 + mt) * 1024 + lane * 16);
#pragma unroll
      for (int j = 0; j < 4; ++j) acc[mt][j] = mm16b(af, bfr[j], acc[mt][j]);
    }
    __builtin_amdgcn_sched_barrier(0);
  }
#pragma unroll
  for (int j = 0; j < 4; ++j) {
    const float tot = swap32_add(swap16_add(ss[j]));
    const float rs = __builtin_amdgcn_rsqf(tot * (1.0f / 512.0f) + kEps), ne = rs * -1.44269504088896341f;
    u32 o[8];
#pragma unroll
    for (int mt = 0; mt < 7; ++mt) {
      const float e0 = __builtin_amdgcn_exp2f(acc[mt][j][0] * ne), e1 = __builtin_amdgcn_exp2f(acc[mt][j][1] * ne);
      o[mt] = pk_bf(acc[mt][j][2] * rs * __builtin_amdgcn_rcpf(1.0f + e0), acc[mt][j][3] * rs * __builtin_amdgcn_rcpf(1.0f + e1));
    }
    o[7] = 0;
    tr4(o[0], o[1], o[2], o[3]);
    tr4(o[4], o[5], o[6], o[7]);
    const u32 off = HP_OFF + (u32)((a * 4 + w) * 4 + j) * (224 * 256) + 7 * fb * 256 + lane * 16;
    st128<AUX>(u4{o[0], o[1], o[2], o[3]}, ws, off, 0);
    if (g < 3) st128<AUX>(u4{o[4], o[5], o[6], o[7]}, ws, off, 1024);
  }
}

template <int AUX> DI void phase5(const Args& p, LAS char* L, rsrc_t ws, int b, int t) {
  const int lane = t & 63, w = __builtin_amdgcn_readfirstlane(t >> 6), m = lane & 15, g = lane >> 4;
  const int a = b & 7, tg = b >> 6, T = (b >> 3) & 7;
  const rsrc_t rwd = mk_rsrc(p.wd, kF * kD * 2);
  f4 acc[4][4];
#pragma unroll
  for (int dt = 0; dt < 4; ++dt)
#pragma unroll
    for (int j = 0; j < 4; ++j) acc[dt][j] = f4{0.f, 0.f, 0.f, 0.f};
  const int stok = 32 * (4 * (2 * a + (tg >> 1)) + w) + 16 * (tg & 1) + m;
  const u4* xp = (const u4*)(p.x + (size_t)stok * kD + 64 * T + 16 * g);
  const u4 x0 = xp[0], x1 = xp[1];
  {
    const u32 voff = YH_OFF + g * 8192 + (a * 64 + 16 * tg + m) * 16;
#pragma unroll
    for (int kq = 0; kq < 4; ++kq) {
      const int q = 4 * w + kq;
      u4 yl[4];
#pragma unroll
      for (int i = 0; i < 4; ++i) yl[i] = ld128<AUX>(ws, voff, kap_off(q, i));
      const int k0 = 4 * (q & 7) + 128 * (q >> 3) + g;
#pragma unroll
      for (int dt = 0; dt < 4; ++dt) {
        const int d = 64 * T + 4 * m + dt;
        u4 af;
#pragma unroll
        for (int i = 0; i < 4; ++i) {
          const int kap = k0 + 32 * i;
          const float rev = (float)((kap * d) & 511) * (1.0f / 512.0f);
          float c = cosr(rev) * 0.0625f, s = sinr(rev) * -0.0625f;
          if (i == 0 && kap == 0) { c = (d & 1) ? -0.04419417382415922f : 0.04419417382415922f; s = 0.f; }
          af[i] = pk_bf(c, s);
        }
#pragma unroll
        for (int j = 0; j < 4; ++j) acc[dt][j] = mm16b(af, u4{yl[0][j], yl[1][j], yl[2][j], yl[3][j]}, acc[dt][j]);
      }
    }
  }
  const u32 hoff = HP_OFF + (u32)((a * 4 + tg) * 4) * (224 * 256) + 56 * w * 256 + lane * 16;
  const u32 woff = (u32)(8 * (56 * w + g)) * 1024 + (64 * T + 4 * m) * 2;
#pragma unroll
  for (int ks = 0; ks < 14; ++ks) {
    u2 wl[8];
#pragma unroll
    for (int e = 0; e < 8; ++e) wl[e] = __builtin_amdgcn_raw_buffer_load_b64(rwd, woff + (e & 3) * 1024, ks * 32768 + (e >> 2) * 4096, 0);
    u4 bfr[4];
#pragma unroll
    for (int j = 0; j < 4; ++j) bfr[j] = ld128<AUX>(ws, hoff, j * (224 * 256) + ks * 1024);
#pragma unroll
    for (int dt = 0; dt < 4; ++dt) {
      u4 af;
#pragma unroll
      for (int pp = 0; pp < 4; ++pp) af[pp] = __builtin_amdgcn_perm(wl[2 * pp + 1][dt >> 1], wl[2 * pp][dt >> 1], (dt & 1) ? 0x07060302u : 0x05040100u);
#pragma unroll
      for (int j = 0; j < 4; ++j) acc[dt][j] = mm16b(af, bfr[j], acc[dt][j]);
    }
  }
  __syncthreads();
#pragma unroll
  for (int j = 0; j < 4; ++j)
#pragma unroll
    for (int dt = 0; dt < 4; ++dt) *(LAS f4*)(L + ((j * 4 + w) * 4 + dt) * 1024 + lane * 16) = acc[dt][j];
  __syncthreads();
  f4 tot[4];
#pragma unroll
  for (int dt = 0; dt < 4; ++dt) {
    tot[dt] = *(LAS f4*)(L + ((w * 4 + 0) * 4 + dt) * 1024 + lane * 16);
#pragma unroll
    for (int sw = 1; sw < 4; ++sw) tot[dt] += *(LAS f4*)(L + ((w * 4 + sw) * 4 + dt) * 1024 + lane * 16);
  }
  u4 o0, o1;
#pragma unroll
  for (int rr = 0; rr < 4; ++rr) {
    const u32 xa = rr < 2 ? x0[2 * rr] : x1[2 * rr - 4], xb = rr < 2 ? x0[2 * rr + 1] : x1[2 * rr - 3];
    const u32 lo = pk_bf(tot[0][rr] + bf_lo(xa), tot[1][rr] + bf_hi(xa)), hi = pk_bf(tot[2][rr] + bf_lo(xb), tot[3][rr] + bf_hi(xb));
    if (rr < 2) { o0[2 * rr] = lo; o0[2 * rr + 1] = hi; } else { o1[2 * rr - 4] = lo; o1[2 * rr - 3] = hi; }
  }
  u4* op = (u4*)(p.out + (size_t)stok * kD + 64 * T + 16 * g);
  op[0] = o0; op[1] = o1;
}

__global__ void __launch_bounds__(256, 1) k1(Args p) {
  extern __shared__ __attribute__((aligned(16))) char smem[];
  LAS char* L = (LAS char*)smem;
  const rsrc_t ws = mk_rsrc(p.ws, WS_BYTES);
  R512<false> kh; R512<true> kb; R512T tw;
  r512_consts(threadIdx.x & 63, kh, kb, tw);
  phase1<0>(p, L, ws, blockIdx.x, threadIdx.x, kh, tw, 0x1234u, 0x56789abcu);
  phase0<0>(p, L, ws, blockIdx.x, threadIdx.x, kb, tw);
}
__global__ void __launch_bounds__(256, 1) k2(Args p) {
  extern __shared__ __attribute__((aligned(16))) char smem[];
  LAS char* L = (LAS char*)smem;
  const rsrc_t ws = mk_rsrc(p.ws, WS_BYTES);
  P2K k;
  p2_consts(threadIdx.x & 63, __builtin_amdgcn_readfirstlane(threadIdx.x >> 6), k);
  phase2<0>(L, ws, blockIdx.x, threadIdx.x, k);
}
__global__ void __launch_bounds__(256, 1) k4(Args p) {
  extern __shared__ __attribute__((aligned(16))) char smem[];
  LAS char* L = (LAS char*)smem;
  const rsrc_t ws = mk_rsrc(p.ws, WS_BYTES);
  load_wp<0>(L, ws, blockIdx.x >> 3, threadIdx.x);
  __syncthreads();
  phase4<0>(L, ws, blockIdx.x, threadIdx.x);
}
__global__ void __launch_bounds__(256, 1) k5(Args p) {
  extern __shared__ __attribute__((aligned(16))) char smem[];
  LAS char* L = (LAS char*)smem;
  const rsrc_t ws = mk_rsrc(p.ws, WS_BYTES);
  phase5<0>(p, L, ws, blockIdx.x, threadIdx.x);
}
}

extern "C" void kernel_launch(void* const* d_in, const int* in_sizes, int n_in, void* d_out, int out_size, void* d_ws, size_t ws_size,
                              hipStream_t stream) {
  Args p;
  p.x = (const u16*)d_in[0]; p.wr = (const u16*)d_in[1]; p.wi = (const u16*)d_in[2];
  p.wg = (const u16*)d_in[4]; p.wu = (const u16*)d_in[5]; p.wd = (const u16*)d_in[6];
  p.out = (u16*)d_out; p.ws = (unsigned char*)d_ws;
  (void)hipFuncSetAttribute((const void*)k1, hipFuncAttributeMaxDynamicSharedMemorySize, L_BYTES);
  (void)hipFuncSetAttribute((const void*)k2, hipFuncAttributeMaxDynamicSharedMemorySize, L_BYTES);
  (void)hipFuncSetAttribute((const void*)k4, hipFuncAttributeMaxDynamicSharedMemorySize, L_BYTES);
  (void)hipFuncSetAttribute((const void*)k5, hipFuncAttributeMaxDynamicSharedMemorySize, L_BYTES);
  k1<<<256, 256, 40960, stream>>>(p);
  k2<<<256, 256, 69632, stream>>>(p);
  k4<<<256, 256, L_BYTES, stream>>>(p);
  k5<<<256, 256, 65536, stream>>>(p);
}
```

```cpp
#include <hip/hip_runtime.h>
#include <stdint.h>

#define LDS_BYTES 147456
#define KERNEL_ASM \
  "s_load_dwordx16 s[4:19], s[0:1], 0x0\n" \
  "v_and_b32 v1, 63, v0\n" \
  "v_lshrrev_b32 v2, 6, v0\n" \
  "s_nop 0\n" \
  "v_readfirstlane_b32 s3, v2\n" \
  "s_and_b32 s64, s2, 0x7\n" \
  "s_lshr_b32 s65, s2, 0x3\n" \
  "s_lshl_b32 s66, s64, 0x5\n" \
  "s_add_u32 s66, s66, s65\n" \
  "s_mul_i32 s68, s64, 0x1c\n" \
  "s_add_u32 s68, s68, s65\n" \
  "s_getpc_b64 s[70:71]\n" \
  "Lpc%=:\n" \
  "s_lshl_b32 s72, s65, 0x2\n" \
  "s_add_u32 s72, s72, s3\n" \
  "s_mov_b32 s73, (Lend%=-Lpc%=)>>10\n" \
  "s_cmp_lt_u32 s72, s73\n" \
  "s_cselect_b32 s72, s72, 0\n" \
  "s_lshl_b32 s72, s72, 0xa\n" \
  "v_lshl_add_u32 v3, v1, 4, s72\n" \
  "global_load_dwordx4 a[0:3], v3, s[70:71]\n" \
  "v_lshrrev_b32 v15, 5, v1\n" \
  "v_bfe_u32 v16, v1, 4, 1\n" \
  "v_and_b32 v17, 15, v1\n" \
  "v_lshlrev_b32 v19, 15, v16\n" \
  "v_lshl_or_b32 v19, v17, 1, v19\n" \
  "v_lshl_or_b32 v19, v15, 7, v19\n" \
  "s_lshl_b32 s62, s64, 0x3\n" \
  "s_lshl_b32 s61, s3, 0x1\n" \
  "s_add_u32 s62, s62, s61\n" \
  "s_lshl_b32 s62, s62, 0x5\n" \
  "s_add_u32 s62, s62, s65\n" \
  "s_lshl_b32 s62, s62, 0xa\n" \
  "s_mov_b32 s42, 0x200000\n" \
  "s_mov_b32 s43, 0x20000\n" \
  "v_mov_b32 v48, 0\n" \
  "v_mov_b32 v49, 0\n" \
  "v_mov_b32 v50, 0\n" \
  "v_mov_b32 v51, 0\n" \
  "v_mov_b32 v52, 0\n" \
  "v_mov_b32 v53, 0\n" \
  "v_mov_b32 v54, 0\n" \
  "v_mov_b32 v55, 0\n" \
  "v_mov_b32 v56, 0\n" \
  "v_mov_b32 v57, 0\n" \
  "v_mov_b32 v58, 0\n" \
  "v_mov_b32 v59, 0\n" \
  "v_mov_b32 v60, 0\n" \
  "v_mov_b32 v61, 0\n" \
  "v_mov_b32 v62, 0\n" \
  "v_mov_b32 v63, 0\n" \
  "v_and_b32 v14, 31, v1\n" \
  "v_lshrrev_b32 v18, 4, v1\n" \
  "v_lshlrev_b32 v20, 1, v0\n" \
  "v_cmp_eq_u32 vcc, 0, v0\n" \
  "v_mov_b32 v2, 0x200\n" \
  "v_bfrev_b32 v12, 1\n" \
  "v_cndmask_b32 v20, v20, v2, vcc\n" \
  "v_cndmask_b32 v12, v12, 0, vcc\n" \
  "v_bfe_u32 v2, v0, 1, 6\n" \
  "v_mul_u32_u24 v21, 0xe00, v2\n" \
  "v_and_b32 v2, 1, v0\n" \
  "v_lshl_or_b32 v21, v2, 4, v21\n" \
  "s_cmp_lt_u32 s3, 2\n" \
  "s_cselect_b32 s60, 0x80000000, 0x0\n" \
  "s_cmp_lt_u32 s65, 28\n" \
  "s_cselect_b32 s61, s60, 0x80000000\n" \
  "v_or_b32 v21, s61, v21\n" \
  "s_cmp_eq_u32 s3, 2\n" \
  "s_cselect_b32 s61, 0x0, 0x80000000\n" \
  "v_bfe_u32 v2, v0, 3, 4\n" \
  "v_lshlrev_b32 v22, 10, v2\n" \
  "v_and_b32 v2, 7, v0\n" \
  "v_lshl_or_b32 v22, v2, 4, v22\n" \
  "v_or_b32 v23, s61, v22\n" \
  "v_or_b32 v22, s60, v22\n" \
  "v_lshlrev_b32 v9, 4, v1\n" \
  "v_cmp_gt_u32 vcc, 56, v1\n" \
  "v_bfrev_b32 v3, 1\n" \
  "s_nop 0\n" \
  "v_cndmask_b32 v9, v3, v9, vcc\n" \
  "v_or_b32 v9, s60, v9\n" \
  "s_lshl_b32 s60, s3, 0x2\n" \
  "v_add_u32 v2, s60, v18\n" \
  "v_lshl_or_b32 v2, v2, 5, v17\n" \
  "v_lshlrev_b32 v24, 4, v2\n" \
  "v_cvt_f32_u32 v10, v14\n" \
  "v_lshlrev_b32 v3, 2, v15\n" \
  "v_cvt_f32_u32 v11, v3\n" \
  "v_mov_b32 v64, -1\n" \
  "v_mov_b32 v65, -1\n" \
  "v_mov_b32 v66, -1\n" \
  "v_mov_b32 v67, -1\n" \
  "v_mul_f32 v3, 0x3d000000, v10\n" \
  "v_mul_f32 v4, v11, v3\n" \
  "v_cos_f32 v84, v4\n" \
  "v_fmamk_f32 v87, v3, 0x3f800000, v4\n" \
  "v_fmamk_f32 v89, v3, 0x40000000, v4\n" \
  "v_fmamk_f32 v91, v3, 0x40400000, v4\n" \
  "v_fmamk_f32 v93, v3, 0x41000000, v4\n" \
  "v_fmamk_f32 v95, v3, 0x41100000, v4\n" \
  "v_fmamk_f32 v97, v3, 0x41200000, v4\n" \
  "v_fmamk_f32 v99, v3, 0x41300000, v4\n" \
  "v_sin_f32 v85, -v4\n" \
  "v_cos_f32 v86, v87\n" \
  "v_cos_f32 v88, v89\n" \
  "v_cos_f32 v90, v91\n" \
  "v_cos_f32 v92, v93\n" \
  "v_cos_f32 v94, v95\n" \
  "v_cos_f32 v96, v97\n" \
  "v_cos_f32 v98, v99\n" \
  "v_sin_f32 v87, -v87\n" \
  "v_sin_f32 v89, -v89\n" \
  "v_sin_f32 v91, -v91\n" \
  "v_sin_f32 v93, -v93\n" \
  "v_sin_f32 v95, -v95\n" \
  "v_sin_f32 v97, -v97\n" \
  "v_sin_f32 v99, -v99\n" \
  "v_cvt_pk_f16_f32 v116, v84, v86\n" \
  "v_cvt_pk_f16_f32 v120, v85, v87\n" \
  "v_cvt_pk_f16_f32 v117, v88, v90\n" \
  "v_cvt_pk_f16_f32 v121, v89, v91\n" \
  "v_cvt_pk_f16_f32 v118, v92, v94\n" \
  "v_cvt_pk_f16_f32 v122, v93, v95\n" \
  "v_cvt_pk_f16_f32 v119, v96, v98\n" \
  "v_cvt_pk_f16_f32 v123, v97, v99\n" \
  "v_cvt_pk_bf16_f32 v32, v84, v86\n" \
  "v_cvt_pk_bf16_f32 v36, v85, v87\n" \
  "v_cvt_pk_bf16_f32 v33, v88, v90\n" \
  "v_cvt_pk_bf16_f32 v37, v89, v91\n" \
  "v_cvt_pk_bf16_f32 v34, v92, v94\n" \
  "v_cvt_pk_bf16_f32 v38, v93, v95\n" \
  "v_cvt_pk_bf16_f32 v35, v96, v98\n" \
  "v_cvt_pk_bf16_f32 v39, v97, v99\n" \
  "v_accvgpr_write_b32 a4, v32\n" \
  "v_accvgpr_write_b32 a5, v33\n" \
  "v_accvgpr_write_b32 a6, v34\n" \
  "v_accvgpr_write_b32 a7, v35\n" \
  "v_accvgpr_write_b32 a8, v36\n" \
  "v_accvgpr_write_b32 a9, v37\n" \
  "v_accvgpr_write_b32 a10, v38\n" \
  "v_accvgpr_write_b32 a11, v39\n" \
  "v_fmamk_f32 v85, v3, 0x41800000, v4\n" \
  "v_fmamk_f32 v87, v3, 0x41880000, v4\n" \
  "v_fmamk_f32 v89, v3, 0x41900000, v4\n" \
  "v_fmamk_f32 v91, v3, 0x41980000, v4\n" \
  "v_fmamk_f32 v93, v3, 0x41c00000, v4\n" \
  "v_fmamk_f32 v95, v3, 0x41c80000, v4\n" \
  "v_fmamk_f32 v97, v3, 0x41d00000, v4\n" \
  "v_fmamk_f32 v99, v3, 0x41d80000, v4\n" \
  "v_cos_f32 v84, v85\n" \
  "v_cos_f32 v86, v87\n" \
  "v_cos_f32 v88, v89\n" \
  "v_cos_f32 v90, v91\n" \
  "v_cos_f32 v92, v93\n" \
  "v_cos_f32 v94, v95\n" \
  "v_cos_f32 v96, v97\n" \
  "v_cos_f32 v98, v99\n" \
  "v_sin_f32 v85, -v85\n" \
  "v_sin_f32 v87, -v87\n" \
  "v_sin_f32 v89, -v89\n" \
  "v_sin_f32 v91, -v91\n" \
  "v_sin_f32 v93, -v93\n" \
  "v_sin_f32 v95, -v95\n" \
  "v_sin_f32 v97, -v97\n" \
  "v_sin_f32 v99, -v99\n" \
  "v_cvt_pk_f16_f32 v124, v84, v86\n" \
  "v_cvt_pk_f16_f32 v128, v85, v87\n" \
  "v_cvt_pk_f16_f32 v125, v88, v90\n" \
  "v_cvt_pk_f16_f32 v129, v89, v91\n" \
  "v_cvt_pk_f16_f32 v126, v92, v94\n" \
  "v_cvt_pk_f16_f32 v130, v93, v95\n" \
  "v_cvt_pk_f16_f32 v127, v96, v98\n" \
  "v_cvt_pk_f16_f32 v131, v97, v99\n" \
  "v_cvt_pk_bf16_f32 v32, v84, v86\n" \
  "v_cvt_pk_bf16_f32 v36, v85, v87\n" \
  "v_cvt_pk_bf16_f32 v33, v88, v90\n" \
  "v_cvt_pk_bf16_f32 v37, v89, v91\n" \
  "v_cvt_pk_bf16_f32 v34, v92, v94\n" \
  "v_cvt_pk_bf16_f32 v38, v93, v95\n" \
  "v_cvt_pk_bf16_f32 v35, v96, v98\n" \
  "v_cvt_pk_bf16_f32 v39, v97, v99\n" \
  "v_accvgpr_write_b32 a12, v32\n" \
  "v_accvgpr_write_b32 a13, v33\n" \
  "v_accvgpr_write_b32 a14, v34\n" \
  "v_accvgpr_write_b32 a15, v35\n" \
  "v_accvgpr_write_b32 a16, v36\n" \
  "v_accvgpr_write_b32 a17, v37\n" \
  "v_accvgpr_write_b32 a18, v38\n" \
  "v_accvgpr_write_b32 a19, v39\n" \
  "v_and_b32 v2, 7, v14\n" \
  "v_cvt_f32_u32 v2, v2\n" \
  "v_mul_f32 v3, 0x3d800000, v2\n" \
  "v_mul_f32 v4, 0x3b000000, v10\n" \
  "v_cmp_gt_u32 s[54:55], 8, v14\n" \
  "v_cmp_gt_u32 s[56:57], 16, v14\n" \
  "v_cmp_eq_u32 s[58:59], 16, v14\n" \
  "v_mul_f32 v5, v11, v3\n" \
  "v_mul_f32 v6, v11, v4\n" \
  "v_cos_f32 v84, v5\n" \
  "v_fmamk_f32 v87, v3, 0x3f800000, v5\n" \
  "v_fmamk_f32 v89, v3, 0x40000000, v5\n" \
  "v_fmamk_f32 v91, v3, 0x40400000, v5\n" \
  "v_fmamk_f32 v93, v3, 0x41000000, v5\n" \
  "v_fmamk_f32 v95, v3, 0x41100000, v5\n" \
  "v_fmamk_f32 v97, v3, 0x41200000, v5\n" \
  "v_fmamk_f32 v99, v3, 0x41300000, v5\n" \
  "v_sin_f32 v85, v5\n" \
  "v_cos_f32 v86, v87\n" \
  "v_cos_f32 v88, v89\n" \
  "v_cos_f32 v90, v91\n" \
  "v_cos_f32 v92, v93\n" \
  "v_cos_f32 v94, v95\n" \
  "v_cos_f32 v96, v97\n" \
  "v_cos_f32 v98, v99\n" \
  "v_sin_f32 v87, v87\n" \
  "v_sin_f32 v89, v89\n" \
  "v_sin_f32 v91, v91\n" \
  "v_sin_f32 v93, v93\n" \
  "v_sin_f32 v95, v95\n" \
  "v_sin_f32 v97, v97\n" \
  "v_sin_f32 v99, v99\n" \
  "v_cos_f32 v68, v6\n" \
  "v_fmamk_f32 v77, v4, 0x3f800000, v6\n" \
  "v_fmamk_f32 v78, v4, 0x40000000, v6\n" \
  "v_fmamk_f32 v79, v4, 0x40400000, v6\n" \
  "v_fmamk_f32 v80, v4, 0x41000000, v6\n" \
  "v_fmamk_f32 v81, v4, 0x41100000, v6\n" \
  "v_fmamk_f32 v82, v4, 0x41200000, v6\n" \
  "v_fmamk_f32 v83, v4, 0x41300000, v6\n" \
  "v_sin_f32 v76, v6\n" \
  "v_cos_f32 v69, v77\n" \
  "v_cos_f32 v70, v78\n" \
  "v_cos_f32 v71, v79\n" \
  "v_cos_f32 v72, v80\n" \
  "v_cos_f32 v73, v81\n" \
  "v_cos_f32 v74, v82\n" \
  "v_cos_f32 v75, v83\n" \
  "v_sin_f32 v77, v77\n" \
  "v_sin_f32 v78, v78\n" \
  "v_sin_f32 v79, v79\n" \
  "v_sin_f32 v80, v80\n" \
  "v_sin_f32 v81, v81\n" \
  "v_sin_f32 v82, v82\n" \
  "v_sin_f32 v83, v83\n" \
  "v_mov_b32 v5, 0x3f3504f3\n" \
  "v_cndmask_b32 v5, 0, v5, s[58:59]\n" \
  "v_sub_f32 v6, 0, v5\n" \
  "v_cndmask_b32 v100, -v85, v84, s[54:55]\n" \
  "v_cndmask_b32 v108, v84, v85, s[54:55]\n" \
  "v_cndmask_b32 v101, -v87, v86, s[54:55]\n" \
  "v_cndmask_b32 v109, v86, v87, s[54:55]\n" \
  "v_cndmask_b32 v102, -v89, v88, s[54:55]\n" \
  "v_cndmask_b32 v110, v88, v89, s[54:55]\n" \
  "v_cndmask_b32 v103, -v91, v90, s[54:55]\n" \
  "v_cndmask_b32 v111, v90, v91, s[54:55]\n" \
  "v_cndmask_b32 v104, -v93, v92, s[54:55]\n" \
  "v_cndmask_b32 v112, v92, v93, s[54:55]\n" \
  "v_cndmask_b32 v105, -v95, v94, s[54:55]\n" \
  "v_cndmask_b32 v113, v94, v95, s[54:55]\n" \
  "v_cndmask_b32 v106, -v97, v96, s[54:55]\n" \
  "v_cndmask_b32 v114, v96, v97, s[54:55]\n" \
  "v_cndmask_b32 v107, -v99, v98, s[54:55]\n" \
  "v_cndmask_b32 v115, v98, v99, s[54:55]\n" \
  "v_cndmask_b32 v100, v5, v100, s[56:57]\n" \
  "v_cndmask_b32 v108, 0, v108, s[56:57]\n" \
  "v_cndmask_b32 v101, v6, v101, s[56:57]\n" \
  "v_cndmask_b32 v109, 0, v109, s[56:57]\n" \
  "v_cndmask_b32 v102, v5, v102, s[56:57]\n" \
  "v_cndmask_b32 v110, 0, v110, s[56:57]\n" \
  "v_cndmask_b32 v103, v6, v103, s[56:57]\n" \
  "v_cndmask_b32 v111, 0, v111, s[56:57]\n" \
  "v_cndmask_b32 v104, v5, v104, s[56:57]\n" \
  "v_cndmask_b32 v112, 0, v112, s[56:57]\n" \
  "v_cndmask_b32 v105, v6, v105, s[56:57]\n" \
  "v_cndmask_b32 v113, 0, v113, s[56:57]\n" \
  "v_cndmask_b32 v106, v5, v106, s[56:57]\n" \
  "v_cndmask_b32 v114, 0, v114, s[56:57]\n" \
  "v_cndmask_b32 v107, v6, v107, s[56:57]\n" \
  "v_cndmask_b32 v115, 0, v115, s[56:57]\n" \
  "v_cvt_pk_f16_f32 v132, v100, v101\n" \
  "v_cvt_pk_f16_f32 v136, v108, v109\n" \
  "v_cvt_pk_f16_f32 v133, v102, v103\n" \
  "v_cvt_pk_f16_f32 v137, v110, v111\n" \
  "v_cvt_pk_f16_f32 v134, v104, v105\n" \
  "v_cvt_pk_f16_f32 v138, v112, v113\n" \
  "v_cvt_pk_f16_f32 v135, v106, v107\n" \
  "v_cvt_pk_f16_f32 v139, v114, v115\n" \
  "v_cvt_pk_bf16_f32 v32, v100, v101\n" \
  "v_cvt_pk_bf16_f32 v36, v108, v109\n" \
  "v_cvt_pk_bf16_f32 v33, v102, v103\n" \
  "v_cvt_pk_bf16_f32 v37, v110, v111\n" \
  "v_cvt_pk_bf16_f32 v34, v104, v105\n" \
  "v_cvt_pk_bf16_f32 v38, v112, v113\n" \
  "v_cvt_pk_bf16_f32 v35, v106, v107\n" \
  "v_cvt_pk_bf16_f32 v39, v114, v115\n" \
  "v_accvgpr_write_b32 a20, v32\n" \
  "v_accvgpr_write_b32 a21, v33\n" \
  "v_accvgpr_write_b32 a22, v34\n" \
  "v_accvgpr_write_b32 a23, v35\n" \
  "v_accvgpr_write_b32 a24, v36\n" \
  "v_accvgpr_write_b32 a25, v37\n" \
  "v_accvgpr_write_b32 a26, v38\n" \
  "v_accvgpr_write_b32 a27, v39\n" \
  "s_mov_b32 s60, 0x3d800000\n" \
  "v_pk_mul_f32 v[68:69], v[68:69], s[60:61] op_sel_hi:[1,0]\n" \
  "v_pk_mul_f32 v[76:77], v[76:77], s[60:61] op_sel_hi:[1,0]\n" \
  "v_pk_mul_f32 v[70:71], v[70:71], s[60:61] op_sel_hi:[1,0]\n" \
  "v_pk_mul_f32 v[78:79], v[78:79], s[60:61] op_sel_hi:[1,0]\n" \
  "v_pk_mul_f32 v[72:73], v[72:73], s[60:61] op_sel_hi:[1,0]\n" \
  "v_pk_mul_f32 v[80:81], v[80:81], s[60:61] op_sel_hi:[1,0]\n" \
  "v_pk_mul_f32 v[74:75], v[74:75], s[60:61] op_sel_hi:[1,0]\n" \
  "v_pk_mul_f32 v[82:83], v[82:83], s[60:61] op_sel_hi:[1,0]\n" \
  "s_lshr_b32 s50, s2, 0x6\n" \
  "s_lshl_b32 s50, s50, 0x4\n" \
  "s_and_b32 s61, s2, 0xf\n" \
  "s_add_u32 s50, s50, s61\n" \
  "s_lshl_b32 s61, s2, 0x4\n" \
  "s_and_b32 s61, s61, 0x300\n" \
  "s_add_u32 s50, s50, s61\n" \
  "s_mul_i32 s50, s50, 0x202\n" \
  "s_mul_i32 s52, s65, 0xe000\n" \
  "s_lshl_b32 s61, s64, 0x7\n" \
  "s_add_u32 s52, s52, s61\n" \
  "s_lshl_b32 s63, s66, 0xd\n" \
  "s_add_u32 s63, s63, 0x800000\n" \
  "s_mov_b32 s38, 0x2000000\n" \
  "s_mov_b32 s39, 0x20000\n" \
  "s_mov_b32 s22, 0x101000\n" \
  "s_mov_b32 s23, 0x20000\n" \
  "s_mov_b32 s26, 0x101000\n" \
  "s_mov_b32 s27, 0x20000\n" \
  "s_mov_b32 s34, 0x1c0000\n" \
  "s_mov_b32 s35, 0x20000\n" \
  "s_mov_b32 s46, 0x200000\n" \
  "s_mov_b32 s47, 0x20000\n" \
  "s_mov_b32 s30, 0x1c0000\n" \
  "s_mov_b32 s31, 0x20000\n" \
  "s_cmp_lt_u32 s68, 112\n" \
  "s_cselect_b32 s51, 0x0, 0x70\n" \
  "s_sub_u32 s51, s68, s51\n" \
  "s_lshl_b32 s51, s51, 0x5\n" \
  "v_mov_b32 v2, 0x1a00000\n" \
  "s_waitcnt lgkmcnt(0)\n" \
  "s_mov_b32 s40, s4\n" \
  "s_and_b32 s41, s5, 0xffff\n" \
  "buffer_load_short_d16_hi v48, v19, s[40:43], s62 offen\n" \
  "buffer_load_short_d16_hi v49, v19, s[40:43], s62 offen offset:32\n" \
  "buffer_load_short_d16_hi v50, v19, s[40:43], s62 offen offset:64\n" \
  "buffer_load_short_d16_hi v51, v19, s[40:43], s62 offen offset:96\n" \
  "buffer_load_short_d16_hi v52, v19, s[40:43], s62 offen offset:256\n" \
  "buffer_load_short_d16_hi v53, v19, s[40:43], s62 offen offset:288\n" \
  "buffer_load_short_d16_hi v54, v19, s[40:43], s62 offen offset:320\n" \
  "buffer_load_short_d16_hi v55, v19, s[40:43], s62 offen offset:352\n" \
  "buffer_load_short_d16_hi v56, v19, s[40:43], s62 offen offset:512\n" \
  "buffer_load_short_d16_hi v57, v19, s[40:43], s62 offen offset:544\n" \
  "buffer_load_short_d16_hi v58, v19, s[40:43], s62 offen offset:576\n" \
  "buffer_load_short_d16_hi v59, v19, s[40:43], s62 offen offset:608\n" \
  "buffer_load_short_d16_hi v60, v19, s[40:43], s62 offen offset:768\n" \
  "buffer_load_short_d16_hi v61, v19, s[40:43], s62 offen offset:800\n" \
  "buffer_load_short_d16_hi v62, v19, s[40:43], s62 offen offset:832\n" \
  "buffer_load_short_d16_hi v63, v19, s[40:43], s62 offen offset:864\n" \
  "s_mov_b32 s36, s18\n" \
  "s_and_b32 s37, s19, 0xffff\n" \
  "s_mov_b32 s20, s6\n" \
  "s_and_b32 s21, s7, 0xffff\n" \
  "s_mov_b32 s24, s8\n" \
  "s_and_b32 s25, s9, 0xffff\n" \
  "buffer_load_dwordx2 v[26:27], v2, s[36:39], 0 offen sc1\n" \
  "s_add_u32 s53, s50, 0x0\n" \
  "buffer_load_ushort v32, v20, s[20:23], s53 offen\n" \
  "buffer_load_ushort v40, v20, s[24:27], s53 offen\n" \
  "s_add_u32 s53, s50, 0x8080\n" \
  "buffer_load_ushort v33, v20, s[20:23], s53 offen\n" \
  "buffer_load_ushort v41, v20, s[24:27], s53 offen\n" \
  "s_add_u32 s53, s50, 0x10100\n" \
  "buffer_load_ushort v34, v20, s[20:23], s53 offen\n" \
  "buffer_load_ushort v42, v20, s[24:27], s53 offen\n" \
  "s_add_u32 s53, s50, 0x18180\n" \
  "buffer_load_ushort v35, v20, s[20:23], s53 offen\n" \
  "buffer_load_ushort v43, v20, s[24:27], s53 offen\n" \
  "s_add_u32 s53, s50, 0x80800\n" \
  "buffer_load_ushort v36, v20, s[20:23], s53 offen\n" \
  "buffer_load_ushort v44, v20, s[24:27], s53 offen\n" \
  "s_add_u32 s53, s50, 0x88880\n" \
  "buffer_load_ushort v37, v20, s[20:23], s53 offen\n" \
  "buffer_load_ushort v45, v20, s[24:27], s53 offen\n" \
  "s_add_u32 s53, s50, 0x90900\n" \
  "buffer_load_ushort v38, v20, s[20:23], s53 offen\n" \
  "buffer_load_ushort v46, v20, s[24:27], s53 offen\n" \
  "s_add_u32 s53, s50, 0x98980\n" \
  "buffer_load_ushort v39, v20, s[20:23], s53 offen\n" \
  "buffer_load_ushort v47, v20, s[24:27], s53 offen\n" \
  "s_cmp_lt_u32 s64, 4\n" \
  "s_cselect_b32 s28, s10, s12\n" \
  "s_cselect_b32 s29, s11, s13\n" \
  "s_and_b32 s29, s29, 0xffff\n" \
  "s_mov_b32 s32, s14\n" \
  "s_and_b32 s33, s15, 0xffff\n" \
  "s_mov_b32 s44, s16\n" \
  "s_and_b32 s45, s17, 0xffff\n" \
  "s_lshl_b32 s69, s65, 0x1\n" \
  "s_and_b32 s61, s3, 0x1\n" \
  "s_add_u32 s69, s69, s61\n" \
  "s_mul_i32 s69, s69, 0x7000\n" \
  "s_and_b32 s61, s64, 0x3\n" \
  "s_mul_i32 s61, s61, 0x380\n" \
  "s_add_u32 s69, s69, s61\n" \
  "v_lshlrev_b32 v3, 21, v1\n" \
  "v_cmp_gt_u32 vcc, 13, v1\n" \
  "v_bfrev_b32 v4, 1\n" \
  "s_nop 0\n" \
  "v_cndmask_b32 v3, v4, v3, vcc\n" \
  "s_cmp_lt_u32 s3, 2\n" \
  "s_cselect_b32 s61, 0x80000000, 0x0\n" \
  "v_or_b32 v3, s61, v3\n" \
  "v_mov_b32 v30, s61\n" \
  "buffer_load_dword a108, v3, s[36:39], 0 offen\n" \
  "buffer_load_dword a109, v30, s[44:47], 0 offen\n" \
  "v_lshrrev_b32 v7, 2, v0\n" \
  "buffer_load_dwordx4 a[76:79], v9, s[28:31], s69 offen\n" \
  "v_and_b32 v8, 3, v0\n" \
  "s_add_u32 s69, s69, 0xe00\n" \
  "buffer_load_dwordx4 a[80:83], v9, s[28:31], s69 offen\n" \
  "v_lshlrev_b32 v88, 4, v0\n" \
  "s_add_u32 s69, s69, 0xe00\n" \
  "buffer_load_dwordx4 a[84:87], v9, s[28:31], s69 offen\n" \
  "v_lshlrev_b32 v102, 2, v8\n" \
  "s_add_u32 s69, s69, 0xe00\n" \
  "buffer_load_dwordx4 a[88:91], v9, s[28:31], s69 offen\n" \
  "v_bfe_u32 v2, v14, 2, 2\n" \
  "s_add_u32 s69, s69, 0xe00\n" \
  "buffer_load_dwordx4 a[92:95], v9, s[28:31], s69 offen\n" \
  "v_xor_b32 v2, s3, v2\n" \
  "v_lshlrev_b32 v84, 5, v14\n" \
  "v_lshl_or_b32 v84, v15, 12, v84\n" \
  "s_add_u32 s69, s69, 0xe00\n" \
  "buffer_load_dwordx4 a[96:99], v9, s[28:31], s69 offen\n" \
  "v_lshl_or_b32 v84, v2, 3, v84\n" \
  "v_bfe_u32 v2, v0, 4, 2\n" \
  "v_xor_b32 v2, v2, v8\n" \
  "s_add_u32 s69, s69, 0xe00\n" \
  "buffer_load_dwordx4 a[100:103], v9, s[28:31], s69 offen\n" \
  "v_lshlrev_b32 v85, 5, v7\n" \
  "v_lshl_or_b32 v85, v2, 3, v85\n" \
  "v_and_b32 v3, 3, v7\n" \
  "s_add_u32 s69, s69, 0xe00\n" \
  "buffer_load_dwordx4 a[104:107], v9, s[28:31], s69 offen\n" \
  "v_lshlrev_b32 v86, 5, v0\n" \
  "v_lshl_or_b32 v86, v3, 3, v86\n" \
  "s_lshl_b32 s60, s3, 0x1\n" \
  "buffer_load_dwordx4 a[60:63], v22, s[32:35], s52 offen\n" \
  "v_add_u32 v2, s60, v16\n" \
  "v_lshlrev_b32 v87, 2, v2\n" \
  "v_cmp_eq_u32 s[58:59], 0, v1\n" \
  "s_add_u32 s52, s52, 0x4000\n" \
  "buffer_load_dwordx4 a[64:67], v22, s[32:35], s52 offen\n" \
  "v_bfe_u32 v2, v0, 3, 2\n" \
  "v_and_b32 v3, 1, v0\n" \
  "v_lshlrev_b32 v3, 1, v3\n" \
  "s_add_u32 s52, s52, 0x4000\n" \
  "buffer_load_dwordx4 a[68:71], v22, s[32:35], s52 offen\n" \
  "v_xor_b32 v2, v2, v3\n" \
  "v_bfe_u32 v3, v0, 1, 6\n" \
  "v_lshlrev_b32 v89, 5, v3\n" \
  "s_add_u32 s52, s52, 0x4000\n" \
  "buffer_load_dwordx4 a[72:75], v23, s[32:35], s52 offen\n" \
  "v_lshl_or_b32 v89, v2, 3, v89\n" \
  "v_xor_b32 v90, 8, v89\n" \
  "v_bfe_u32 v2, v17, 2, 2\n" \
  "buffer_load_dwordx4 a[28:31], v21, s[28:31], s51 offen\n" \
  "v_xor_b32 v3, s3, v2\n" \
  "v_lshl_or_b32 v4, v15, 6, v17\n" \
  "v_lshlrev_b32 v91, 5, v4\n" \
  "v_lshl_or_b32 v91, v3, 3, v91\n" \
  "v_lshl_or_b32 v91, v16, 2, v91\n" \
  "v_lshlrev_b32 v94, 7, v2\n" \
  "v_lshl_or_b32 v94, v18, 5, v94\n" \
  "v_lshl_or_b32 v94, v8, 3, v94\n" \
  "s_and_b32 s60, s3, 0x1\n" \
  "s_lshl_b32 s60, s60, 0xc\n" \
  "v_add_u32 v94, s60, v94\n" \
  "v_lshl_or_b32 v3, v18, 2, v8\n" \
  "v_lshlrev_b32 v3, 4, v3\n" \
  "v_lshl_or_b32 v95, v2, 8, v3\n" \
  "s_lshl_b32 s60, s3, 0x2\n" \
  "v_and_b32 v2, 7, v14\n" \
  "v_xor_b32 v2, s60, v2\n" \
  "v_lshlrev_b32 v92, 8, v1\n" \
  "v_lshl_or_b32 v92, v2, 4, v92\n" \
  "v_bfe_u32 v3, v0, 4, 3\n" \
  "v_xor_b32 v2, v3, v17\n" \
  "v_lshlrev_b32 v93, 8, v3\n" \
  "v_lshl_or_b32 v93, v2, 4, v93\n" \
  "v_and_b32 v5, 31, v7\n" \
  "v_lshlrev_b32 v96, 14, v5\n" \
  "v_lshl_or_b32 v96, v8, 4, v96\n" \
  "v_lshlrev_b32 v98, 4, v5\n" \
  "s_add_u32 s51, s51, 0x38000\n" \
  "buffer_load_dwordx4 a[32:35], v21, s[28:31], s51 offen\n" \
  "v_lshl_or_b32 v98, v8, 1, v98\n" \
  "v_add_u32 v98, 0x20000, v98\n" \
  "v_lshlrev_b32 v99, 5, v5\n" \
  "v_lshl_or_b32 v99, v8, 3, v99\n" \
  "v_add_u32 v99, 0x1e000, v99\n" \
  "v_and_b32 v2, 7, v7\n" \
  "v_lshrrev_b32 v4, 1, v8\n" \
  "v_lshl_or_b32 v2, v2, 1, v4\n" \
  "v_lshrrev_b32 v4, 5, v0\n" \
  "v_lshl_or_b32 v2, v2, 5, v4\n" \
  "v_and_b32 v3, 1, v8\n" \
  "v_lshlrev_b32 v97, 4, v2\n" \
  "v_lshl_or_b32 v97, v3, 3, v97\n" \
  "v_add_u32 v97, 0x1c000, v97\n" \
  "v_lshl_or_b32 v2, v18, 5, v17\n" \
  "v_lshlrev_b32 v100, 4, v2\n" \
  "v_add_u32 v100, 0x1c000, v100\n" \
  "v_lshlrev_b32 v101, 4, v1\n" \
  "v_add_u32 v101, 0x21000, v101\n" \
  "s_lshl_b32 s60, s3, 0xb\n" \
  "v_add_u32 v103, s60, v101\n" \
  "v_add_u32 v28, s60, v100\n" \
  "v_lshlrev_b32 v25, 5, v0\n" \
  "v_add_u32 v25, 0x1e000, v25\n" \
  "s_lshl_b32 s60, s3, 0x1\n" \
  "v_lshrrev_b32 v2, 1, v18\n" \
  "v_add_u32 v2, s60, v2\n" \
  "s_add_u32 s51, s51, 0x38000\n" \
  "buffer_load_dwordx4 a[36:39], v21, s[28:31], s51 offen\n" \
  "v_lshl_or_b32 v2, v17, 3, v2\n" \
  "v_and_b32 v3, 1, v18\n" \
  "v_lshlrev_b32 v29, 4, v2\n" \
  "v_lshl_or_b32 v29, v3, 3, v29\n" \
  "v_add_u32 v29, 0x20000, v29\n" \
  "s_lshr_b32 s70, s64, 0x2\n" \
  "s_lshl_b32 s70, s70, 0x2\n" \
  "s_lshr_b32 s72, s65, 0x3\n" \
  "s_add_u32 s70, s70, s72\n" \
  "s_and_b32 s71, s64, 0x3\n" \
  "s_lshl_b32 s71, s71, 0x3\n" \
  "s_and_b32 s73, s65, 0x7\n" \
  "s_add_u32 s71, s71, s73\n" \
  "s_mul_i32 s74, s71, 0x7\n" \
  "s_mul_i32 s75, s64, 0xe0\n" \
  "s_mul_i32 s62, s65, 0x7\n" \
  "s_add_u32 s75, s75, s62\n" \
  "s_and_b32 s62, s3, 0x1\n" \
  "s_lshl_b32 s62, s62, 0x2\n" \
  "s_add_u32 s75, s75, s62\n" \
  "s_lshl_b32 s75, s75, 0xa\n" \
  "s_add_u32 s75, s75, 0x1800000\n" \
  "s_cmp_eq_u32 s66, 0\n" \
  "s_cselect_b32 s76, 0xffff, -0x1\n" \
  "s_mul_i32 s11, s3, 0x3800\n" \
  "s_lshl_b32 s77, s68, 0xe\n" \
  "s_add_u32 s77, s77, 0xc00000\n" \
  "s_add_u32 s51, s51, 0x38000\n" \
  "buffer_load_dwordx4 a[40:43], v21, s[28:31], s51 offen\n" \
  "s_add_u32 s62, s74, 0x0\n" \
  "s_lshr_b32 s4, s62, 0x1\n" \
  "s_lshl_b32 s4, s4, 0xe\n" \
  "s_and_b32 s62, s62, 0x1\n" \
  "s_lshl_b32 s62, s62, 0x7\n" \
  "s_add_u32 s4, s4, s62\n" \
  "s_add_u32 s62, s74, 0x1\n" \
  "s_lshr_b32 s5, s62, 0x1\n" \
  "s_lshl_b32 s5, s5, 0xe\n" \
  "s_and_b32 s62, s62, 0x1\n" \
  "s_lshl_b32 s62, s62, 0x7\n" \
  "s_add_u32 s5, s5, s62\n" \
  "s_add_u32 s62, s74, 0x2\n" \
  "s_lshr_b32 s6, s62, 0x1\n" \
  "s_lshl_b32 s6, s6, 0xe\n" \
  "s_and_b32 s62, s62, 0x1\n" \
  "s_lshl_b32 s62, s62, 0x7\n" \
  "s_add_u32 s6, s6, s62\n" \
  "s_add_u32 s62, s74, 0x3\n" \
  "s_lshr_b32 s7, s62, 0x1\n" \
  "s_lshl_b32 s7, s7, 0xe\n" \
  "s_and_b32 s62, s62, 0x1\n" \
  "s_lshl_b32 s62, s62, 0x7\n" \
  "s_add_u32 s7, s7, s62\n" \
  "s_add_u32 s62, s74, 0x4\n" \
  "s_lshr_b32 s8, s62, 0x1\n" \
  "s_lshl_b32 s8, s8, 0xe\n" \
  "s_add_u32 s51, s51, 0x38000\n" \
  "buffer_load_dwordx4 a[44:47], v21, s[28:31], s51 offen\n" \
  "s_and_b32 s62, s62, 0x1\n" \
  "s_lshl_b32 s62, s62, 0x7\n" \
  "s_add_u32 s8, s8, s62\n" \
  "s_add_u32 s62, s74, 0x5\n" \
  "s_lshr_b32 s9, s62, 0x1\n" \
  "s_lshl_b32 s9, s9, 0xe\n" \
  "s_and_b32 s62, s62, 0x1\n" \
  "s_lshl_b32 s62, s62, 0x7\n" \
  "s_add_u32 s9, s9, s62\n" \
  "s_add_u32 s62, s74, 0x6\n" \
  "s_lshr_b32 s10, s62, 0x1\n" \
  "s_lshl_b32 s10, s10, 0xe\n" \
  "s_and_b32 s62, s62, 0x1\n" \
  "s_lshl_b32 s62, s62, 0x7\n" \
  "s_add_u32 s10, s10, s62\n" \
  "s_lshl_b32 s62, s3, 0xb\n" \
  "s_add_u32 s62, s62, 0xc00000\n" \
  "s_add_u32 s4, s4, s62\n" \
  "s_add_u32 s5, s5, s62\n" \
  "s_add_u32 s6, s6, s62\n" \
  "s_add_u32 s7, s7, s62\n" \
  "s_add_u32 s8, s8, s62\n" \
  "s_add_u32 s9, s9, s62\n" \
  "s_add_u32 s10, s10, s62\n" \
  "s_waitcnt vmcnt(36)\n" \
  "v_pk_add_f32 v[188:189], v[48:49], v[50:51]\n" \
  "v_pk_add_f32 v[190:191], v[52:53], v[54:55]\n" \
  "v_pk_add_f32 v[192:193], v[56:57], v[58:59]\n" \
  "v_pk_add_f32 v[194:195], v[60:61], v[62:63]\n" \
  "v_pk_add_f32 v[188:189], v[188:189], v[190:191]\n" \
  "v_pk_add_f32 v[192:193], v[192:193], v[194:195]\n" \
  "v_pk_add_f32 v[188:189], v[188:189], v[192:193]\n" \
  "v_add_f32 v188, v188, v189\n" \
  "s_nop 1\n" \
  "v_add_f32_dpp v188, v188, v188 row_ror:8 row_mask:0xf bank_mask:0xf\n" \
  "s_nop 1\n" \
  "v_add_f32_dpp v188, v188, v188 row_ror:4 row_mask:0xf bank_mask:0xf\n" \
  "s_nop 1\n" \
  "v_add_f32_dpp v188, v188, v188 row_ror:2 row_mask:0xf bank_mask:0xf\n" \
  "s_nop 1\n" \
  "v_add_f32_dpp v188, v188, v188 row_ror:1 row_mask:0xf bank_mask:0xf\n" \
  "v_mov_b32 v189, v188\n" \
  "s_nop 1\n" \
  "v_permlane32_swap_b32 v188, v189\n" \
  "v_add_f32 v188, v188, v189\n" \
  "v_mul_f32 v188, 0x3b000000, v188\n" \
  "v_pk_add_f32 v[48:49], v[48:49], v[188:189] op_sel_hi:[1,0] neg_lo:[0,1] neg_hi:[0,1]\n" \
  "v_pk_add_f32 v[50:51], v[50:51], v[188:189] op_sel_hi:[1,0] neg_lo:[0,1] neg_hi:[0,1]\n" \
  "v_pk_add_f32 v[52:53], v[52:53], v[188:189] op_sel_hi:[1,0] neg_lo:[0,1] neg_hi:[0,1]\n" \
  "v_pk_add_f32 v[54:55], v[54:55], v[188:189] op_sel_hi:[1,0] neg_lo:[0,1] neg_hi:[0,1]\n" \
  "v_pk_add_f32 v[56:57], v[56:57], v[188:189] op_sel_hi:[1,0] neg_lo:[0,1] neg_hi:[0,1]\n" \
  "v_pk_add_f32 v[58:59], v[58:59], v[188:189] op_sel_hi:[1,0] neg_lo:[0,1] neg_hi:[0,1]\n" \
  "v_pk_add_f32 v[60:61], v[60:61], v[188:189] op_sel_hi:[1,0] neg_lo:[0,1] neg_hi:[0,1]\n" \
  "v_pk_add_f32 v[62:63], v[62:63], v[188:189] op_sel_hi:[1,0] neg_lo:[0,1] neg_hi:[0,1]\n" \
  "v_pk_mul_f32 v[192:193], v[48:49], v[48:49]\n" \
  "s_add_u32 s51, s51, 0x38000\n" \
  "buffer_load_dwordx4 a[48:51], v21, s[28:31], s51 offen\n" \
  "v_pk_mul_f32 v[194:195], v[50:51], v[50:51]\n" \
  "v_pk_fma_f32 v[192:193], v[52:53], v[52:53], v[192:193]\n" \
  "v_pk_fma_f32 v[194:195], v[54:55], v[54:55], v[194:195]\n" \
  "v_pk_fma_f32 v[192:193], v[56:57], v[56:57], v[192:193]\n" \
  "v_pk_fma_f32 v[194:195], v[58:59], v[58:59], v[194:195]\n" \
  "v_pk_fma_f32 v[192:193], v[60:61], v[60:61], v[192:193]\n" \
  "v_pk_fma_f32 v[194:195], v[62:63], v[62:63], v[194:195]\n" \
  "v_pk_add_f32 v[192:193], v[192:193], v[194:195]\n" \
  "v_add_f32 v192, v192, v193\n" \
  "s_nop 1\n" \
  "v_add_f32_dpp v192, v192, v192 row_ror:8 row_mask:0xf bank_mask:0xf\n" \
  "s_nop 1\n" \
  "v_add_f32_dpp v192, v192, v192 row_ror:4 row_mask:0xf bank_mask:0xf\n" \
  "s_nop 1\n" \
  "v_add_f32_dpp v192, v192, v192 row_ror:2 row_mask:0xf bank_mask:0xf\n" \
  "s_nop 1\n" \
  "v_add_f32_dpp v192, v192, v192 row_ror:1 row_mask:0xf bank_mask:0xf\n" \
  "v_mov_b32 v193, v192\n" \
  "s_nop 1\n" \
  "v_permlane32_swap_b32 v192, v193\n" \
  "v_add_f32 v192, v192, v193\n" \
  "v_mul_f32 v192, 0x3b000000, v192\n" \
  "v_add_f32 v192, 0x3727c5ac, v192\n" \
  "v_rsq_f32 v194, v192\n" \
  "s_nop 0\n" \
  "v_pk_mul_f32 v[48:49], v[48:49], v[194:195] op_sel_hi:[1,0]\n" \
  "v_pk_mul_f32 v[50:51], v[50:51], v[194:195] op_sel_hi:[1,0]\n" \
  "v_pk_mul_f32 v[52:53], v[52:53], v[194:195] op_sel_hi:[1,0]\n" \
  "v_pk_mul_f32 v[54:55], v[54:55], v[194:195] op_sel_hi:[1,0]\n" \
  "v_pk_mul_f32 v[56:57], v[56:57], v[194:195] op_sel_hi:[1,0]\n" \
  "v_pk_mul_f32 v[58:59], v[58:59], v[194:195] op_sel_hi:[1,0]\n" \
  "v_pk_mul_f32 v[60:61], v[60:61], v[194:195] op_sel_hi:[1,0]\n" \
  "v_pk_mul_f32 v[62:63], v[62:63], v[194:195] op_sel_hi:[1,0]\n" \
  "v_cvt_pk_f16_f32 v220, v48, v49\n" \
  "v_cvt_pk_f16_f32 v221, v50, v51\n" \
  "s_add_u32 s51, s51, 0x38000\n" \
  "buffer_load_dwordx4 a[52:55], v21, s[28:31], s51 offen\n" \
  "v_cvt_pk_f16_f32 v222, v52, v53\n" \
  "v_cvt_pk_f16_f32 v223, v54, v55\n" \
  "v_cvt_pk_f16_f32 v224, v56, v57\n" \
  "v_cvt_pk_f16_f32 v225, v58, v59\n" \
  "v_cvt_pk_f16_f32 v226, v60, v61\n" \
  "v_cvt_pk_f16_f32 v227, v62, v63\n" \
  "s_add_u32 s51, s51, 0x38000\n" \
  "buffer_load_dwordx4 a[56:59], v21, s[28:31], s51 offen\n" \
  "v_mfma_f32_32x32x16_f16 v[140:155], v[220:223], v[116:119], 0\n" \
  "v_mfma_f32_32x32x16_f16 v[156:171], v[220:223], v[120:123], 0\n" \
  "v_mfma_f32_32x32x16_f16 v[140:155], v[224:227], v[124:127], v[140:155]\n" \
  "v_mfma_f32_32x32x16_f16 v[156:171], v[224:227], v[128:131], v[156:171]\n" \
  "v_mul_f32 v193, v192, v194\n" \
  "v_cvt_pk_bf16_f32 v193, v193, 0\n" \
  "ds_write_b32 v87, v193 offset:16384\n" \
  "s_waitcnt vmcnt(22)\n" \
  "s_cmp_lt_u32 s3, 2\n" \
  "s_cbranch_scc0 LC_b%=\n" \
  "buffer_store_dwordx4 v[64:67], v88, s[36:39], s63 offen sc1\n" \
  "buffer_store_dwordx4 v[64:67], v88, s[36:39], s63 offen offset:2048 sc1\n" \
  "s_add_u32 s63, s63, 0x1000\n" \
  "buffer_store_dwordx4 v[64:67], v88, s[36:39], s63 offen sc1\n" \
  "buffer_store_dwordx4 v[64:67], v88, s[36:39], s63 offen offset:2048 sc1\n" \
  "LC_b%=:\n" \
  "v_lshl_or_b32 v32, v40, 16, v32\n" \
  "v_lshl_or_b32 v33, v41, 16, v33\n" \
  "v_lshl_or_b32 v34, v42, 16, v34\n" \
  "v_lshl_or_b32 v35, v43, 16, v35\n" \
  "v_lshl_or_b32 v36, v44, 16, v36\n" \
  "v_lshl_or_b32 v37, v45, 16, v37\n" \
  "v_lshl_or_b32 v38, v46, 16, v38\n" \
  "v_lshl_or_b32 v39, v47, 16, v39\n" \
  "ds_write_b64 v86, v[32:33] offset:8192\n" \
  "v_xor_b32 v3, 8, v86\n" \
  "ds_write_b64 v3, v[34:35] offset:8192\n" \
  "v_xor_b32 v4, 16, v86\n" \
  "ds_write_b64 v4, v[36:37] offset:8192\n" \
  "v_xor_b32 v5, 24, v86\n" \
  "ds_write_b64 v5, v[38:39] offset:8192\n" \
  "s_waitcnt vmcnt(38)\n" \
  "v_readfirstlane_b32 s48, v26\n" \
  "v_readfirstlane_b32 s61, v27\n" \
  "s_add_u32 s48, s48, 0x1\n" \
  "s_mul_i32 s49, s48, 0x9e3779b1\n" \
  "s_xor_b32 s49, s49, s61\n" \
  "s_and_b32 s67, s49, 0xffff0000\n" \
  "s_lshl_b32 s60, s2, 0xe\n" \
  "v_mov_b32 v51, s48\n" \
  "v_mov_b32 v55, s48\n" \
  "v_mov_b32 v59, s48\n" \
  "v_mov_b32 v63, s48\n" \
  "v_mov_b32 v10, s48\n" \
  "v_mov_b32 v11, s49\n" \
  "v_pk_mul_f32 v[188:189], v[156:157], v[76:77]\n" \
  "v_pk_mul_f32 v[192:193], v[158:159], v[78:79]\n" \
  "v_pk_mul_f32 v[196:197], v[160:161], v[80:81]\n" \
  "v_pk_mul_f32 v[200:201], v[162:163], v[82:83]\n" \
  "v_pk_mul_f32 v[190:191], v[140:141], v[76:77]\n" \
  "v_pk_mul_f32 v[194:195], v[142:143], v[78:79]\n" \
  "v_pk_mul_f32 v[198:199], v[144:145], v[80:81]\n" \
  "v_pk_mul_f32 v[202:203], v[146:147], v[82:83]\n" \
  "v_pk_fma_f32 v[188:189], v[140:141], v[68:69], v[188:189]\n" \
  "v_pk_fma_f32 v[192:193], v[142:143], v[70:71], v[192:193]\n" \
  "v_pk_fma_f32 v[196:197], v[144:145], v[72:73], v[196:197]\n" \
  "v_pk_fma_f32 v[200:201], v[146:147], v[74:75], v[200:201]\n" \
  "v_pk_fma_f32 v[190:191], v[156:157], v[68:69], v[190:191] neg_lo:[0,0,1] neg_hi:[0,0,1]\n" \
  "v_pk_fma_f32 v[194:195], v[158:159], v[70:71], v[194:195] neg_lo:[0,0,1] neg_hi:[0,0,1]\n" \
  "v_pk_fma_f32 v[198:199], v[160:161], v[72:73], v[198:199] neg_lo:[0,0,1] neg_hi:[0,0,1]\n" \
  "v_pk_fma_f32 v[202:203], v[162:163], v[74:75], v[202:203] neg_lo:[0,0,1] neg_hi:[0,0,1]\n" \
  "v_cvt_pk_f16_f32 v204, v188, v189\n" \
  "v_cvt_pk_f16_f32 v205, v192, v193\n" \
  "v_cvt_pk_f16_f32 v206, v196, v197\n" \
  "v_cvt_pk_f16_f32 v207, v200, v201\n" \
  "v_cvt_pk_f16_f32 v208, v190, v191\n" \
  "v_cvt_pk_f16_f32 v209, v194, v195\n" \
  "v_cvt_pk_f16_f32 v210, v198, v199\n" \
  "v_cvt_pk_f16_f32 v211, v202, v203\n" \
  "v_mfma_f32_32x32x16_f16 v[172:187], v[132:135], v[204:207], 0\n" \
  "s_nop 0\n" \
  "v_mfma_f32_32x32x16_f16 v[172:187], v[136:139], v[208:211], v[172:187]\n" \
  "v_pk_mul_f32 v[188:189], v[164:165], v[76:77]\n" \
  "v_pk_mul_f32 v[192:193], v[166:167], v[78:79]\n" \
  "v_pk_mul_f32 v[196:197], v[168:169], v[80:81]\n" \
  "v_pk_mul_f32 v[200:201], v[170:171], v[82:83]\n" \
  "v_pk_mul_f32 v[190:191], v[148:149], v[76:77]\n" \
  "v_pk_mul_f32 v[194:195], v[150:151], v[78:79]\n" \
  "v_pk_mul_f32 v[198:199], v[152:153], v[80:81]\n" \
  "v_pk_mul_f32 v[202:203], v[154:155], v[82:83]\n" \
  "v_pk_fma_f32 v[188:189], v[148:149], v[68:69], v[188:189]\n" \
  "v_pk_fma_f32 v[192:193], v[150:151], v[70:71], v[192:193]\n" \
  "v_pk_fma_f32 v[196:197], v[152:153], v[72:73], v[196:197]\n" \
  "v_pk_fma_f32 v[200:201], v[154:155], v[74:75], v[200:201]\n" \
  "v_pk_fma_f32 v[190:191], v[164:165], v[68:69], v[190:191] neg_lo:[0,0,1] neg_hi:[0,0,1]\n" \
  "v_pk_fma_f32 v[194:195], v[166:167], v[70:71], v[194:195] neg_lo:[0,0,1] neg_hi:[0,0,1]\n" \
  "v_pk_fma_f32 v[198:199], v[168:169], v[72:73], v[198:199] neg_lo:[0,0,1] neg_hi:[0,0,1]\n" \
  "v_pk_fma_f32 v[202:203], v[170:171], v[74:75], v[202:203] neg_lo:[0,0,1] neg_hi:[0,0,1]\n" \
  "v_cvt_pk_f16_f32 v212, v188, v189\n" \
  "v_cvt_pk_f16_f32 v213, v192, v193\n" \
  "v_cvt_pk_f16_f32 v214, v196, v197\n" \
  "v_cvt_pk_f16_f32 v215, v200, v201\n" \
  "v_cvt_pk_f16_f32 v216, v190, v191\n" \
  "v_cvt_pk_f16_f32 v217, v194, v195\n" \
  "v_cvt_pk_f16_f32 v218, v198, v199\n" \
  "v_cvt_pk_f16_f32 v219, v202, v203\n" \
  "v_mfma_f32_32x32x16_f16 v[140:155], v[132:135], v[212:215], 0\n" \
  "s_nop 0\n" \
  "v_mfma_f32_32x32x16_f16 v[140:155], v[136:139], v[216:219], v[140:155]\n" \
  "v_cndmask_b32 v172, v172, v180, s[58:59]\n" \
  "v_cndmask_b32 v176, v176, 0, s[58:59]\n" \
  "s_nop 9\n" \
  "v_cndmask_b32 v140, v140, v148, s[58:59]\n" \
  "v_cndmask_b32 v144, v144, 0, s[58:59]\n" \
  "v_cvt_pk_f16_f32 v188, v172, v140\n" \
  "v_cvt_pk_f16_f32 v189, v176, v144\n" \
  "ds_write_b64 v84, v[188:189]\n" \
  "v_cvt_pk_f16_f32 v192, v173, v141\n" \
  "v_cvt_pk_f16_f32 v193, v177, v145\n" \
  "ds_write_b64 v84, v[192:193] offset:1024\n" \
  "v_cvt_pk_f16_f32 v196, v174, v142\n" \
  "v_cvt_pk_f16_f32 v197, v178, v146\n" \
  "ds_write_b64 v84, v[196:197] offset:2048\n" \
  "v_cvt_pk_f16_f32 v200, v175, v143\n" \
  "v_cvt_pk_f16_f32 v201, v179, v147\n" \
  "ds_write_b64 v84, v[200:201] offset:3072\n" \
  "s_waitcnt lgkmcnt(0)\n" \
  "s_barrier\n" \
  "s_cmp_lt_u32 s3, 2\n" \
  "s_cbranch_scc0 LR_b%=\n" \
  "ds_read_b32 v8, v102 offset:16384\n" \
  "ds_read_b32 v9, v102 offset:16400\n" \
  "ds_read_b64 v[48:49], v85\n" \
  "ds_read_b64 v[52:53], v85 offset:1024\n" \
  "ds_read_b64 v[56:57], v85 offset:2048\n" \
  "ds_read_b64 v[60:61], v85 offset:3072\n" \
  "ds_read_b64 v[188:189], v85 offset:4096\n" \
  "ds_read_b64 v[192:193], v85 offset:5120\n" \
  "v_mov_b32 v191, s48\n" \
  "v_mov_b32 v195, s48\n" \
  "v_mov_b32 v199, s48\n" \
  "v_mov_b32 v203, s48\n" \
  "s_waitcnt lgkmcnt(5)\n" \
  "ds_read_b64 v[196:197], v85 offset:6144\n" \
  "ds_read_b64 v[200:201], v85 offset:7168\n" \
  "v_or_b32 v50, s67, v8\n" \
  "v_or_b32 v54, s67, v8\n" \
  "v_or_b32 v58, s67, v8\n" \
  "v_or_b32 v62, s67, v8\n" \
  "v_or_b32 v190, s67, v8\n" \
  "v_or_b32 v194, s67, v8\n" \
  "v_or_b32 v198, s67, v8\n" \
  "v_or_b32 v202, s67, v8\n" \
  "v_or_b32 v9, s67, v9\n" \
  "s_waitcnt lgkmcnt(7)\n" \
  "buffer_store_dwordx4 v[48:51], v88, s[36:39], s60 offen sc1\n" \
  "ds_read_b64 a[112:113], v85 offset:8192\n" \
  "v_accvgpr_write_b32 a114, v9\n" \
  "v_accvgpr_write_b32 a115, v10\n" \
  "s_waitcnt lgkmcnt(7)\n" \
  "buffer_store_dwordx4 v[52:55], v88, s[36:39], s60 offen offset:2048 sc1\n" \
  "s_add_u32 s60, s60, 0x1000\n" \
  "ds_read_b64 a[116:117], v85 offset:9216\n" \
  "v_accvgpr_write_b32 a118, v9\n" \
  "v_accvgpr_write_b32 a119, v10\n" \
  "s_waitcnt lgkmcnt(7)\n" \
  "buffer_store_dwordx4 v[56:59], v88, s[36:39], s60 offen sc1\n" \
  "ds_read_b64 a[120:121], v85 offset:10240\n" \
  "v_accvgpr_write_b32 a122, v9\n" \
  "v_accvgpr_write_b32 a123, v10\n" \
  "s_waitcnt lgkmcnt(7)\n" \
  "buffer_store_dwordx4 v[60:63], v88, s[36:39], s60 offen offset:2048 sc1\n" \
  "s_add_u32 s60, s60, 0x1000\n" \
  "ds_read_b64 a[124:125], v85 offset:11264\n" \
  "v_accvgpr_write_b32 a126, v9\n" \
  "v_accvgpr_write_b32 a127, v10\n" \
  "s_waitcnt lgkmcnt(7)\n" \
  "buffer_store_dwordx4 v[188:191], v88, s[36:39], s60 offen sc1\n" \
  "ds_read_b64 a[128:129], v85 offset:12288\n" \
  "v_accvgpr_write_b32 a130, v9\n" \
  "v_accvgpr_write_b32 a131, v10\n" \
  "s_waitcnt lgkmcnt(7)\n" \
  "buffer_store_dwordx4 v[192:195], v88, s[36:39], s60 offen offset:2048 sc1\n" \
  "s_add_u32 s60, s60, 0x1000\n" \
  "ds_read_b64 a[132:133], v85 offset:13312\n" \
  "v_accvgpr_write_b32 a134, v9\n" \
  "v_accvgpr_write_b32 a135, v10\n" \
  "s_waitcnt lgkmcnt(7)\n" \
  "buffer_store_dwordx4 v[196:199], v88, s[36:39], s60 offen sc1\n" \
  "ds_read_b64 a[136:137], v85 offset:14336\n" \
  "v_accvgpr_write_b32 a138, v9\n" \
  "v_accvgpr_write_b32 a139, v10\n" \
  "s_waitcnt lgkmcnt(7)\n" \
  "buffer_store_dwordx4 v[200:203], v88, s[36:39], s60 offen offset:2048 sc1\n" \
  "s_add_u32 s60, s60, 0x1000\n" \
  "ds_read_b64 a[140:141], v85 offset:15360\n" \
  "v_accvgpr_write_b32 a142, v9\n" \
  "v_accvgpr_write_b32 a143, v10\n" \
  "s_add_u32 s60, s60, 0x3fc000\n" \
  "s_waitcnt lgkmcnt(0)\n" \
  "s_branch LR_e%=\n" \
  "LR_b%=:\n" \
  "v_subrev_u32 v2, 0x800, v88\n" \
  "s_waitcnt vmcnt(0)\n" \
  "ds_write_b128 v2, a[60:63] offset:53248\n" \
  "ds_write_b128 v2, a[64:67] offset:55296\n" \
  "ds_write_b128 v2, a[68:71] offset:57344\n" \
  "ds_write_b128 v2, a[72:75] offset:59392\n" \
  "s_waitcnt vmcnt(7)\n" \
  "ds_write_b64 v89, a[28:29] offset:20480\n" \
  "ds_write_b64 v90, a[30:31] offset:20480\n" \
  "s_waitcnt vmcnt(6)\n" \
  "ds_write_b64 v89, a[32:33] offset:22528\n" \
  "ds_write_b64 v90, a[34:35] offset:22528\n" \
  "s_waitcnt vmcnt(5)\n" \
  "ds_write_b64 v89, a[36:37] offset:24576\n" \
  "ds_write_b64 v90, a[38:39] offset:24576\n" \
  "s_waitcnt vmcnt(4)\n" \
  "ds_write_b64 v89, a[40:41] offset:26624\n" \
  "ds_write_b64 v90, a[42:43] offset:26624\n" \
  "s_waitcnt vmcnt(3)\n" \
  "ds_write_b64 v89, a[44:45] offset:28672\n" \
  "ds_write_b64 v90, a[46:47] offset:28672\n" \
  "s_waitcnt vmcnt(2)\n" \
  "ds_write_b64 v89, a[48:49] offset:30720\n" \
  "ds_write_b64 v90, a[50:51] offset:30720\n" \
  "s_waitcnt vmcnt(1)\n" \
  "ds_write_b64 v89, a[52:53] offset:32768\n" \
  "ds_write_b64 v90, a[54:55] offset:32768\n" \
  "s_waitcnt vmcnt(0)\n" \
  "ds_write_b64 v89, a[56:57] offset:34816\n" \
  "ds_write_b64 v90, a[58:59] offset:34816\n" \
  "LR_e%=:\n" \
  "s_cmp_lt_u32 s3, 2\n" \
  "s_cselect_b32 s21, 0x0, 0x400000\n" \
  "s_cselect_b32 s22, 0x0, 0x8\n" \
  "s_lshl_b32 s20, s66, 0x6\n" \
  "s_add_u32 s20, s20, s21\n" \
  "v_add_u32 v98, s22, v98\n" \
  "s_add_u32 s21, s20, 0x80000\n" \
  "s_add_u32 s22, s20, 0x100000\n" \
  "s_add_u32 s23, s20, 0x180000\n" \
  "s_add_u32 s24, s20, 0x200000\n" \
  "s_add_u32 s25, s20, 0x280000\n" \
  "s_add_u32 s26, s20, 0x300000\n" \
  "s_add_u32 s27, s20, 0x380000\n" \
  "s_mov_b32 s61, 0x5040100\n" \
  "s_mov_b32 s62, 0x7060302\n" \
  "v_bfrev_b32 v86, 1\n" \
  "v_cndmask_b32 v86, v86, 0, s[58:59]\n" \
  "s_waitcnt lgkmcnt(0)\n" \
  "s_barrier\n" \
  "ds_read_b32 v228, v91 offset:20480\n" \
  "ds_read_b32 v229, v91 offset:20992\n" \
  "ds_read_b32 v230, v91 offset:21504\n" \
  "ds_read_b32 v231, v91 offset:22016\n" \
  "ds_read_b32 v232, v91 offset:24576\n" \
  "ds_read_b32 v233, v91 offset:25088\n" \
  "ds_read_b32 v234, v91 offset:25600\n" \
  "ds_read_b32 v235, v91 offset:26112\n" \
  "ds_read_b32 v236, v91 offset:28672\n" \
  "ds_read_b32 v237, v91 offset:29184\n" \
  "ds_read_b32 v238, v91 offset:29696\n" \
  "ds_read_b32 v239, v91 offset:30208\n" \
  "ds_read_b32 v240, v91 offset:32768\n" \
  "ds_read_b32 v241, v91 offset:33280\n" \
  "ds_read_b32 v242, v91 offset:33792\n" \
  "ds_read_b32 v243, v91 offset:34304\n" \
  "s_waitcnt lgkmcnt(8)\n" \
  "v_perm_b32 v220, v229, v228, s61\n" \
  "v_perm_b32 v221, v231, v230, s61\n" \
  "v_perm_b32 v222, v233, v232, s61\n" \
  "v_perm_b32 v223, v235, v234, s61\n" \
  "s_waitcnt lgkmcnt(0)\n" \
  "v_perm_b32 v224, v237, v236, s61\n" \
  "v_perm_b32 v225, v239, v238, s61\n" \
  "v_perm_b32 v226, v241, v240, s61\n" \
  "v_perm_b32 v227, v243, v242, s61\n" \
  "v_mfma_f32_32x32x16_bf16 v[140:155], v[220:223], a[4:7], 0\n" \
  "s_cmp_lt_u32 s3, 2\n" \
  "s_cbranch_scc0 LW0_b%=\n" \
  "s_waitcnt vmcnt(8)\n" \
  "buffer_store_dwordx4 a[112:115], v88, s[36:39], s60 offen sc1\n" \
  "buffer_store_dwordx4 a[116:119], v88, s[36:39], s60 offen offset:2048 sc1\n" \
  "s_add_u32 s60, s60, 0x1000\n" \
  "LW0_b%=:\n" \
  "v_mfma_f32_32x32x16_bf16 v[156:171], v[220:223], a[8:11], 0\n" \
  "s_cmp_lt_u32 s3, 2\n" \
  "s_cbranch_scc0 LW1_b%=\n" \
  "buffer_store_dwordx4 a[120:123], v88, s[36:39], s60 offen sc1\n" \
  "buffer_store_dwordx4 a[124:127], v88, s[36:39], s60 offen offset:2048 sc1\n" \
  "s_add_u32 s60, s60, 0x1000\n" \
  "LW1_b%=:\n" \
  "v_mfma_f32_32x32x16_bf16 v[140:155], v[224:227], a[12:15], v[140:155]\n" \
  "s_cmp_lt_u32 s3, 2\n" \
  "s_cbranch_scc0 LW2_b%=\n" \
  "buffer_store_dwordx4 a[128:131], v88, s[36:39], s60 offen sc1\n" \
  "buffer_store_dwordx4 a[132:135], v88, s[36:39], s60 offen offset:2048 sc1\n" \
  "s_add_u32 s60, s60, 0x1000\n" \
  "LW2_b%=:\n" \
  "v_mfma_f32_32x32x16_bf16 v[156:171], v[224:227], a[16:19], v[156:171]\n" \
  "s_cmp_lt_u32 s3, 2\n" \
  "s_cbranch_scc0 LW3_b%=\n" \
  "buffer_store_dwordx4 a[136:139], v88, s[36:39], s60 offen sc1\n" \
  "buffer_store_dwordx4 a[140:143], v88, s[36:39], s60 offen offset:2048 sc1\n" \
  "s_add_u32 s60, s60, 0x1000\n" \
  "LW3_b%=:\n" \
  "s_nop 9\n" \
  "v_pk_mul_f32 v[188:189], v[156:157], v[76:77]\n" \
  "v_pk_mul_f32 v[192:193], v[158:159], v[78:79]\n" \
  "v_pk_mul_f32 v[196:197], v[160:161], v[80:81]\n" \
  "v_pk_mul_f32 v[200:201], v[162:163], v[82:83]\n" \
  "v_pk_mul_f32 v[190:191], v[140:141], v[76:77]\n" \
  "v_pk_mul_f32 v[194:195], v[142:143], v[78:79]\n" \
  "v_pk_mul_f32 v[198:199], v[144:145], v[80:81]\n" \
  "v_pk_mul_f32 v[202:203], v[146:147], v[82:83]\n" \
  "v_pk_fma_f32 v[188:189], v[140:141], v[68:69], v[188:189]\n" \
  "v_pk_fma_f32 v[192:193], v[142:143], v[70:71], v[192:193]\n" \
  "v_pk_fma_f32 v[196:197], v[144:145], v[72:73], v[196:197]\n" \
  "v_pk_fma_f32 v[200:201], v[146:147], v[74:75], v[200:201]\n" \
  "v_pk_fma_f32 v[190:191], v[156:157], v[68:69], v[190:191] neg_lo:[0,0,1] neg_hi:[0,0,1]\n" \
  "v_pk_fma_f32 v[194:195], v[158:159], v[70:71], v[194:195] neg_lo:[0,0,1] neg_hi:[0,0,1]\n" \
  "v_pk_fma_f32 v[198:199], v[160:161], v[72:73], v[198:199] neg_lo:[0,0,1] neg_hi:[0,0,1]\n" \
  "v_pk_fma_f32 v[202:203], v[162:163], v[74:75], v[202:203] neg_lo:[0,0,1] neg_hi:[0,0,1]\n" \
  "v_cvt_pk_bf16_f32 v204, v188, v189\n" \
  "v_cvt_pk_bf16_f32 v205, v192, v193\n" \
  "v_cvt_pk_bf16_f32 v206, v196, v197\n" \
  "v_cvt_pk_bf16_f32 v207, v200, v201\n" \
  "v_cvt_pk_bf16_f32 v208, v190, v191\n" \
  "v_cvt_pk_bf16_f32 v209, v194, v195\n" \
  "v_cvt_pk_bf16_f32 v210, v198, v199\n" \
  "v_cvt_pk_bf16_f32 v211, v202, v203\n" \
  "v_mfma_f32_32x32x16_bf16 v[172:187], a[20:23], v[204:207], 0\n" \
  "s_nop 0\n" \
  "v_mfma_f32_32x32x16_bf16 v[172:187], a[24:27], v[208:211], v[172:187]\n" \
  "v_pk_mul_f32 v[188:189], v[164:165], v[76:77]\n" \
  "v_pk_mul_f32 v[192:193], v[166:167], v[78:79]\n" \
  "v_pk_mul_f32 v[196:197], v[168:169], v[80:81]\n" \
  "v_pk_mul_f32 v[200:201], v[170:171], v[82:83]\n" \
  "v_pk_mul_f32 v[190:191], v[148:149], v[76:77]\n" \
  "v_pk_mul_f32 v[194:195], v[150:151], v[78:79]\n" \
  "v_pk_mul_f32 v[198:199], v[152:153], v[80:81]\n" \
  "v_pk_mul_f32 v[202:203], v[154:155], v[82:83]\n" \
  "v_pk_fma_f32 v[188:189], v[148:149], v[68:69], v[188:189]\n" \
  "v_pk_fma_f32 v[192:193], v[150:151], v[70:71], v[192:193]\n" \
  "v_pk_fma_f32 v[196:197], v[152:153], v[72:73], v[196:197]\n" \
  "v_pk_fma_f32 v[200:201], v[154:155], v[74:75], v[200:201]\n" \
  "v_pk_fma_f32 v[190:191], v[164:165], v[68:69], v[190:191] neg_lo:[0,0,1] neg_hi:[0,0,1]\n" \
  "v_pk_fma_f32 v[194:195], v[166:167], v[70:71], v[194:195] neg_lo:[0,0,1] neg_hi:[0,0,1]\n" \
  "v_pk_fma_f32 v[198:199], v[168:169], v[72:73], v[198:199] neg_lo:[0,0,1] neg_hi:[0,0,1]\n" \
  "v_pk_fma_f32 v[202:203], v[170:171], v[74:75], v[202:203] neg_lo:[0,0,1] neg_hi:[0,0,1]\n" \
  "v_cvt_pk_bf16_f32 v212, v188, v189\n" \
  "v_cvt_pk_bf16_f32 v213, v192, v193\n" \
  "v_cvt_pk_bf16_f32 v214, v196, v197\n" \
  "v_cvt_pk_bf16_f32 v215, v200, v201\n" \
  "v_cvt_pk_bf16_f32 v216, v190, v191\n" \
  "v_cvt_pk_bf16_f32 v217, v194, v195\n" \
  "v_cvt_pk_bf16_f32 v218, v198, v199\n" \
  "v_cvt_pk_bf16_f32 v219, v202, v203\n" \
  "v_mfma_f32_32x32x16_bf16 v[140:155], a[20:23], v[212:215], 0\n" \
  "s_nop 0\n" \
  "v_mfma_f32_32x32x16_bf16 v[140:155], a[24:27], v[216:219], v[140:155]\n" \
  "v_perm_b32 v220, v229, v228, s62\n" \
  "v_perm_b32 v221, v231, v230, s62\n" \
  "v_perm_b32 v222, v233, v232, s62\n" \
  "v_perm_b32 v223, v235, v234, s62\n" \
  "v_perm_b32 v224, v237, v236, s62\n" \
  "v_perm_b32 v225, v239, v238, s62\n" \
  "v_perm_b32 v226, v241, v240, s62\n" \
  "v_perm_b32 v227, v243, v242, s62\n" \
  "v_cndmask_b32 v172, v172, v180, s[58:59]\n" \
  "v_cndmask_b32 v176, v176, 0, s[58:59]\n" \
  "v_cvt_pk_bf16_f32 v188, v172, v176\n" \
  "v_cvt_pk_bf16_f32 v189, v173, v177\n" \
  "v_cvt_pk_bf16_f32 v190, v174, v178\n" \
  "v_cvt_pk_bf16_f32 v191, v175, v179\n" \
  "ds_write_b128 v92, v[188:191] offset:36864\n" \
  "v_cndmask_b32 v140, v140, v148, s[58:59]\n" \
  "v_cndmask_b32 v144, v144, 0, s[58:59]\n" \
  "v_cvt_pk_bf16_f32 v192, v140, v144\n" \
  "v_cvt_pk_bf16_f32 v193, v141, v145\n" \
  "v_cvt_pk_bf16_f32 v194, v142, v146\n" \
  "v_cvt_pk_bf16_f32 v195, v143, v147\n" \
  "v_xor_b32 v4, 32, v92\n" \
  "ds_write_b128 v4, v[192:195] offset:36864\n" \
  "v_mfma_f32_32x32x16_bf16 v[140:155], v[220:223], a[4:7], 0\n" \
  "v_mfma_f32_32x32x16_bf16 v[156:171], v[220:223], a[8:11], 0\n" \
  "v_mfma_f32_32x32x16_bf16 v[140:155], v[224:227], a[12:15], v[140:155]\n" \
  "v_mfma_f32_32x32x16_bf16 v[156:171], v[224:227], a[16:19], v[156:171]\n" \
  "s_nop 11\n" \
  "v_pk_mul_f32 v[188:189], v[156:157], v[76:77]\n" \
  "v_pk_mul_f32 v[192:193], v[158:159], v[78:79]\n" \
  "v_pk_mul_f32 v[196:197], v[160:161], v[80:81]\n" \
  "v_pk_mul_f32 v[200:201], v[162:163], v[82:83]\n" \
  "v_pk_mul_f32 v[190:191], v[140:141], v[76:77]\n" \
  "v_pk_mul_f32 v[194:195], v[142:143], v[78:79]\n" \
  "v_pk_mul_f32 v[198:199], v[144:145], v[80:81]\n" \
  "v_pk_mul_f32 v[202:203], v[146:147], v[82:83]\n" \
  "v_pk_fma_f32 v[188:189], v[140:141], v[68:69], v[188:189]\n" \
  "v_pk_fma_f32 v[192:193], v[142:143], v[70:71], v[192:193]\n" \
  "v_pk_fma_f32 v[196:197], v[144:145], v[72:73], v[196:197]\n" \
  "v_pk_fma_f32 v[200:201], v[146:147], v[74:75], v[200:201]\n" \
  "v_pk_fma_f32 v[190:191], v[156:157], v[68:69], v[190:191] neg_lo:[0,0,1] neg_hi:[0,0,1]\n" \
  "v_pk_fma_f32 v[194:195], v[158:159], v[70:71], v[194:195] neg_lo:[0,0,1] neg_hi:[0,0,1]\n" \
  "v_pk_fma_f32 v[198:199], v[160:161], v[72:73], v[198:199] neg_lo:[0,0,1] neg_hi:[0,0,1]\n" \
  "v_pk_fma_f32 v[202:203], v[162:163], v[74:75], v[202:203] neg_lo:[0,0,1] neg_hi:[0,0,1]\n" \
  "v_cvt_pk_bf16_f32 v204, v188, v189\n" \
  "v_cvt_pk_bf16_f32 v205, v192, v193\n" \
  "v_cvt_pk_bf16_f32 v206, v196, v197\n" \
  "v_cvt_pk_bf16_f32 v207, v200, v201\n" \
  "v_cvt_pk_bf16_f32 v208, v190, v191\n" \
  "v_cvt_pk_bf16_f32 v209, v194, v195\n" \
  "v_cvt_pk_bf16_f32 v210, v198, v199\n" \
  "v_cvt_pk_bf16_f32 v211, v202, v203\n" \
  "v_mfma_f32_32x32x16_bf16 v[172:187], a[20:23], v[204:207], 0\n" \
  "s_nop 0\n" \
  "v_mfma_f32_32x32x16_bf16 v[172:187], a[24:27], v[208:211], v[172:187]\n" \
  "v_pk_mul_f32 v[188:189], v[164:165], v[76:77]\n" \
  "v_pk_mul_f32 v[192:193], v[166:167], v[78:79]\n" \
  "v_pk_mul_f32 v[196:197], v[168:169], v[80:81]\n" \
  "v_pk_mul_f32 v[200:201], v[170:171], v[82:83]\n" \
  "v_pk_mul_f32 v[190:191], v[148:149], v[76:77]\n" \
  "v_pk_mul_f32 v[194:195], v[150:151], v[78:79]\n" \
  "v_pk_mul_f32 v[198:199], v[152:153], v[80:81]\n" \
  "v_pk_mul_f32 v[202:203], v[154:155], v[82:83]\n" \
  "v_pk_fma_f32 v[188:189], v[148:149], v[68:69], v[188:189]\n" \
  "v_pk_fma_f32 v[192:193], v[150:151], v[70:71], v[192:193]\n" \
  "v_pk_fma_f32 v[196:197], v[152:153], v[72:73], v[196:197]\n" \
  "v_pk_fma_f32 v[200:201], v[154:155], v[74:75], v[200:201]\n" \
  "v_pk_fma_f32 v[190:191], v[164:165], v[68:69], v[190:191] neg_lo:[0,0,1] neg_hi:[0,0,1]\n" \
  "v_pk_fma_f32 v[194:195], v[166:167], v[70:71], v[194:195] neg_lo:[0,0,1] neg_hi:[0,0,1]\n" \
  "v_pk_fma_f32 v[198:199], v[168:169], v[72:73], v[198:199] neg_lo:[0,0,1] neg_hi:[0,0,1]\n" \
  "v_pk_fma_f32 v[202:203], v[170:171], v[74:75], v[202:203] neg_lo:[0,0,1] neg_hi:[0,0,1]\n" \
  "v_cvt_pk_bf16_f32 v212, v188, v189\n" \
  "v_cvt_pk_bf16_f32 v213, v192, v193\n" \
  "v_cvt_pk_bf16_f32 v214, v196, v197\n" \
  "v_cvt_pk_bf16_f32 v215, v200, v201\n" \
  "v_cvt_pk_bf16_f32 v216, v190, v191\n" \
  "v_cvt_pk_bf16_f32 v217, v194, v195\n" \
  "v_cvt_pk_bf16_f32 v218, v198, v199\n" \
  "v_cvt_pk_bf16_f32 v219, v202, v203\n" \
  "v_mfma_f32_32x32x16_bf16 v[140:155], a[20:23], v[212:215], 0\n" \
  "s_nop 0\n" \
  "v_mfma_f32_32x32x16_bf16 v[140:155], a[24:27], v[216:219], v[140:155]\n" \
  "v_cndmask_b32 v172, v172, v180, s[58:59]\n" \
  "v_cndmask_b32 v176, v176, 0, s[58:59]\n" \
  "v_cvt_pk_bf16_f32 v188, v172, v176\n" \
  "v_cvt_pk_bf16_f32 v189, v173, v177\n" \
  "v_cvt_pk_bf16_f32 v190, v174, v178\n" \
  "v_cvt_pk_bf16_f32 v191, v175, v179\n" \
  "v_xor_b32 v3, 16, v92\n" \
  "ds_write_b128 v3, v[188:191] offset:36864\n" \
  "s_nop 3\n" \
  "v_cndmask_b32 v140, v140, v148, s[58:59]\n" \
  "v_cndmask_b32 v144, v144, 0, s[58:59]\n" \
  "v_cvt_pk_bf16_f32 v192, v140, v144\n" \
  "v_cvt_pk_bf16_f32 v193, v141, v145\n" \
  "v_cvt_pk_bf16_f32 v194, v142, v146\n" \
  "v_cvt_pk_bf16_f32 v195, v143, v147\n" \
  "v_xor_b32 v5, 48, v92\n" \
  "ds_write_b128 v5, v[192:195] offset:36864\n" \
  "s_waitcnt lgkmcnt(0)\n" \
  "s_barrier\n" \
  "v_cvt_f32_u32 v19, v17\n" \
  "v_lshlrev_b32 v6, 2, v18\n" \
  "v_cvt_f32_u32 v20, v6\n" \
  "s_lshl_b32 s69, s3, 0x4\n" \
  "v_cvt_f32_u32 v6, s69\n" \
  "v_add_f32 v26, v6, v19\n" \
  "v_add_f32 v22, v6, v20\n" \
  "v_mul_f32 v6, 0x3c800000, v26\n" \
  "v_mul_f32 v7, v20, v6\n" \
  "v_cos_f32 v188, v7\n" \
  "s_cmp_lt_u32 s3, 2\n" \
  "s_cbranch_scc0 LL_b%=\n" \
  "buffer_load_dwordx4 v[244:247], v96, s[36:39], s20 offen sc1\n" \
  "buffer_load_dwordx4 v[248:251], v96, s[36:39], s21 offen sc1\n" \
  "buffer_load_dwordx4 v[252:255], v96, s[36:39], s22 offen sc1\n" \
  "buffer_load_dwordx4 v[104:107], v96, s[36:39], s23 offen sc1\n" \
  "buffer_load_dwordx4 v[108:111], v96, s[36:39], s24 offen sc1\n" \
  "buffer_load_dwordx4 v[112:115], v96, s[36:39], s25 offen sc1\n" \
  "buffer_load_dwordx4 v[64:67], v96, s[36:39], s26 offen sc1\n" \
  "buffer_load_dwordx4 v[32:35], v96, s[36:39], s27 offen sc1\n" \
  "LL_b%=:\n" \
  "v_fmamk_f32 v191, v6, 0x3f800000, v7\n" \
  "v_fmamk_f32 v193, v6, 0x40000000, v7\n" \
  "v_fmamk_f32 v195, v6, 0x40400000, v7\n" \
  "v_fmamk_f32 v197, v6, 0x41800000, v7\n" \
  "v_fmamk_f32 v199, v6, 0x41880000, v7\n" \
  "v_fmamk_f32 v201, v6, 0x41900000, v7\n" \
  "v_fmamk_f32 v203, v6, 0x41980000, v7\n" \
  "v_sin_f32 v189, v7\n" \
  "v_cos_f32 v190, v191\n" \
  "v_cos_f32 v192, v193\n" \
  "v_cos_f32 v194, v195\n" \
  "v_cos_f32 v196, v197\n" \
  "v_cos_f32 v198, v199\n" \
  "v_cos_f32 v200, v201\n" \
  "v_cos_f32 v202, v203\n" \
  "v_sin_f32 v191, v191\n" \
  "v_sin_f32 v193, v193\n" \
  "v_sin_f32 v195, v195\n" \
  "v_sin_f32 v197, v197\n" \
  "v_sin_f32 v199, v199\n" \
  "v_sin_f32 v201, v201\n" \
  "v_sin_f32 v203, v203\n" \
  "v_cvt_pk_f16_f32 v36, v188, v190\n" \
  "v_cvt_pk_f16_f32 v37, v189, v191\n" \
  "v_cvt_pk_f16_f32 v38, v192, v194\n" \
  "v_cvt_pk_f16_f32 v39, v193, v195\n" \
  "v_cvt_pk_f16_f32 v52, -v189, -v191\n" \
  "v_cvt_pk_f16_f32 v53, v188, v190\n" \
  "v_cvt_pk_f16_f32 v54, -v193, -v195\n" \
  "v_cvt_pk_f16_f32 v55, v192, v194\n" \
  "v_cvt_pk_bf16_f32 v124, v188, v190\n" \
  "v_cvt_pk_bf16_f32 v125, v189, v191\n" \
  "v_cvt_pk_bf16_f32 v126, v192, v194\n" \
  "v_cvt_pk_bf16_f32 v127, v193, v195\n" \
  "v_cvt_pk_bf16_f32 v136, -v189, -v191\n" \
  "v_cvt_pk_bf16_f32 v137, v188, v190\n" \
  "v_cvt_pk_bf16_f32 v138, -v193, -v195\n" \
  "v_cvt_pk_bf16_f32 v139, v192, v194\n" \
  "v_cvt_pk_f16_f32 v40, v196, v198\n" \
  "v_cvt_pk_f16_f32 v41, v197, v199\n" \
  "v_cvt_pk_f16_f32 v42, v200, v202\n" \
  "v_cvt_pk_f16_f32 v43, v201, v203\n" \
  "v_cvt_pk_f16_f32 v56, -v197, -v199\n" \
  "v_cvt_pk_f16_f32 v57, v196, v198\n" \
  "v_cvt_pk_f16_f32 v58, -v201, -v203\n" \
  "v_cvt_pk_f16_f32 v59, v200, v202\n" \
  "v_cvt_pk_bf16_f32 v120, v196, v198\n" \
  "v_cvt_pk_bf16_f32 v121, v197, v199\n" \
  "v_cvt_pk_bf16_f32 v122, v200, v202\n" \
  "v_cvt_pk_bf16_f32 v123, v201, v203\n" \
  "v_cvt_pk_bf16_f32 v140, -v197, -v199\n" \
  "v_cvt_pk_bf16_f32 v141, v196, v198\n" \
  "v_cvt_pk_bf16_f32 v142, -v201, -v203\n" \
  "v_cvt_pk_bf16_f32 v143, v200, v202\n" \
  "v_fmamk_f32 v189, v6, 0x42000000, v7\n" \
  "v_fmamk_f32 v191, v6, 0x42040000, v7\n" \
  "v_fmamk_f32 v193, v6, 0x42080000, v7\n" \
  "v_fmamk_f32 v195, v6, 0x420c0000, v7\n" \
  "v_fmamk_f32 v197, v6, 0x42400000, v7\n" \
  "v_fmamk_f32 v199, v6, 0x42440000, v7\n" \
  "v_fmamk_f32 v201, v6, 0x42480000, v7\n" \
  "v_fmamk_f32 v203, v6, 0x424c0000, v7\n" \
  "v_cos_f32 v188, v189\n" \
  "v_cos_f32 v190, v191\n" \
  "v_cos_f32 v192, v193\n" \
  "v_cos_f32 v194, v195\n" \
  "v_cos_f32 v196, v197\n" \
  "v_cos_f32 v198, v199\n" \
  "v_cos_f32 v200, v201\n" \
  "v_cos_f32 v202, v203\n" \
  "v_sin_f32 v189, v189\n" \
  "v_sin_f32 v191, v191\n" \
  "v_sin_f32 v193, v193\n" \
  "v_sin_f32 v195, v195\n" \
  "v_sin_f32 v197, v197\n" \
  "v_sin_f32 v199, v199\n" \
  "v_sin_f32 v201, v201\n" \
  "v_sin_f32 v203, v203\n" \
  "v_cvt_pk_f16_f32 v44, v188, v190\n" \
  "v_cvt_pk_f16_f32 v45, v189, v191\n" \
  "v_cvt_pk_f16_f32 v46, v192, v194\n" \
  "v_cvt_pk_f16_f32 v47, v193, v195\n" \
  "v_cvt_pk_f16_f32 v60, -v189, -v191\n" \
  "v_cvt_pk_f16_f32 v61, v188, v190\n" \
  "v_cvt_pk_f16_f32 v62, -v193, -v195\n" \
  "v_cvt_pk_f16_f32 v63, v192, v194\n" \
  "v_cvt_pk_bf16_f32 v128, v188, v190\n" \
  "v_cvt_pk_bf16_f32 v129, v189, v191\n" \
  "v_cvt_pk_bf16_f32 v130, v192, v194\n" \
  "v_cvt_pk_bf16_f32 v131, v193, v195\n" \
  "s_cmp_lt_u32 s3, 2\n" \
  "s_cbranch_scc1 LS0_e%=\n" \
  "ds_read_b128 a[144:147], v93 offset:36864\n" \
  "ds_read_b128 a[148:151], v93 offset:38912\n" \
  "ds_read_b128 a[152:155], v93 offset:40960\n" \
  "ds_read_b128 a[156:159], v93 offset:43008\n" \
  "ds_read_b128 a[160:163], v93 offset:45056\n" \
  "ds_read_b128 a[164:167], v93 offset:47104\n" \
  "ds_read_b128 a[168:171], v93 offset:49152\n" \
  "ds_read_b128 a[172:175], v93 offset:51200\n" \
  "s_cmp_lt_u32 s65, 28\n" \
  "s_cselect_b32 s12, 0x0, 0x80000000\n" \
  "v_subrev_u32 v21, 0x800, v88\n" \
  "v_or_b32 v21, s12, v21\n" \
  "s_cmp_lt_u32 s3, 3\n" \
  "s_cselect_b32 s12, 0x0, 0x80000000\n" \
  "v_or_b32 v9, s12, v95\n" \
  "s_waitcnt lgkmcnt(7)\n" \
  "ds_read_b64_tr_b16 a[176:177], v94 offset:53248\n" \
  "ds_read_b64_tr_b16 a[178:179], v94 offset:53760\n" \
  "ds_read_b64_tr_b16 a[180:181], v94 offset:54272\n" \
  "ds_read_b64_tr_b16 a[182:183], v94 offset:54784\n" \
  "ds_read_b64_tr_b16 a[184:185], v94 offset:55296\n" \
  "ds_read_b64_tr_b16 a[186:187], v94 offset:55808\n" \
  "ds_read_b64_tr_b16 a[188:189], v94 offset:56320\n" \
  "ds_read_b64_tr_b16 a[190:191], v94 offset:56832\n" \
  "s_waitcnt lgkmcnt(15)\n" \
  "buffer_store_dwordx4 a[144:147], v21, s[36:39], s77 offen sc1\n" \
  "s_waitcnt lgkmcnt(14)\n" \
  "buffer_store_dwordx4 a[148:151], v21, s[36:39], s77 offen offset:2048 sc1\n" \
  "s_add_u32 s77, s77, 0x1000\n" \
  "s_waitcnt lgkmcnt(13)\n" \
  "buffer_store_dwordx4 a[152:155], v21, s[36:39], s77 offen sc1\n" \
  "s_waitcnt lgkmcnt(12)\n" \
  "buffer_store_dwordx4 a[156:159], v21, s[36:39], s77 offen offset:2048 sc1\n" \
  "s_add_u32 s77, s77, 0x1000\n" \
  "s_waitcnt lgkmcnt(0)\n" \
  "LS0_e%=:\n" \
  "v_cvt_pk_bf16_f32 v144, -v189, -v191\n" \
  "v_cvt_pk_bf16_f32 v145, v188, v190\n" \
  "v_cvt_pk_bf16_f32 v146, -v193, -v195\n" \
  "v_cvt_pk_bf16_f32 v147, v192, v194\n" \
  "v_cvt_pk_f16_f32 v48, v196, v198\n" \
  "v_cvt_pk_f16_f32 v49, v197, v199\n" \
  "v_cvt_pk_f16_f32 v50, v200, v202\n" \
  "v_cvt_pk_f16_f32 v51, v201, v203\n" \
  "v_cvt_pk_f16_f32 v116, -v197, -v199\n" \
  "v_cvt_pk_f16_f32 v117, v196, v198\n" \
  "s_cmp_lt_u32 s3, 2\n" \
  "s_cbranch_scc1 LS1x_e%=\n" \
  "buffer_store_dwordx4 a[160:163], v21, s[36:39], s77 offen sc1\n" \
  "buffer_store_dwordx4 a[164:167], v21, s[36:39], s77 offen offset:2048 sc1\n" \
  "s_add_u32 s77, s77, 0x1000\n" \
  "LS1x_e%=:\n" \
  "v_cvt_pk_f16_f32 v118, -v201, -v203\n" \
  "v_cvt_pk_f16_f32 v119, v200, v202\n" \
  "v_cvt_pk_bf16_f32 v132, v196, v198\n" \
  "v_cvt_pk_bf16_f32 v133, v197, v199\n" \
  "v_cvt_pk_bf16_f32 v134, v200, v202\n" \
  "v_cvt_pk_bf16_f32 v135, v201, v203\n" \
  "v_cvt_pk_bf16_f32 v148, -v197, -v199\n" \
  "v_cvt_pk_bf16_f32 v149, v196, v198\n" \
  "v_cvt_pk_bf16_f32 v150, -v201, -v203\n" \
  "v_cvt_pk_bf16_f32 v151, v200, v202\n" \
  "s_cmp_lt_u32 s3, 2\n" \
  "s_cbranch_scc1 LS2x_e%=\n" \
  "buffer_store_dwordx4 a[168:171], v21, s[36:39], s77 offen sc1\n" \
  "buffer_store_dwordx4 a[172:175], v21, s[36:39], s77 offen offset:2048 sc1\n" \
  "s_add_u32 s77, s77, 0x1000\n" \
  "LS2x_e%=:\n" \
  "v_mul_f32 v26, 0x3a000000, v26\n" \
  "v_mul_f32 v13, v20, v26\n" \
  "v_cos_f32 v68, v13\n" \
  "v_fmamk_f32 v77, v26, 0x3f800000, v13\n" \
  "v_fmamk_f32 v78, v26, 0x40000000, v13\n" \
  "v_fmamk_f32 v79, v26, 0x40400000, v13\n" \
  "v_fmamk_f32 v80, v26, 0x41800000, v13\n" \
  "v_fmamk_f32 v81, v26, 0x41880000, v13\n" \
  "v_fmamk_f32 v82, v26, 0x41900000, v13\n" \
  "v_fmamk_f32 v83, v26, 0x41980000, v13\n" \
  "v_sin_f32 v76, v13\n" \
  "v_cos_f32 v69, v77\n" \
  "v_cos_f32 v70, v78\n" \
  "v_cos_f32 v71, v79\n" \
  "v_cos_f32 v72, v80\n" \
  "v_cos_f32 v73, v81\n" \
  "v_cos_f32 v74, v82\n" \
  "v_cos_f32 v75, v83\n" \
  "v_sin_f32 v77, v77\n" \
  "v_sin_f32 v78, v78\n" \
  "v_sin_f32 v79, v79\n" \
  "v_sin_f32 v80, v80\n" \
  "v_sin_f32 v81, v81\n" \
  "v_sin_f32 v82, v82\n" \
  "v_sin_f32 v83, v83\n" \
  "v_mul_f32 v6, 0x3d000000, v19\n" \
  "v_mul_f32 v27, v20, v6\n" \
  "v_mul_f32 v26, 0x3a000000, v19\n" \
  "v_mul_f32 v31, v22, v26\n" \
  "v_cos_f32 v188, v27\n" \
  "v_fmamk_f32 v191, v6, 0x3f800000, v27\n" \
  "v_fmamk_f32 v193, v6, 0x40000000, v27\n" \
  "v_fmamk_f32 v195, v6, 0x40400000, v27\n" \
  "v_fmamk_f32 v197, v6, 0x41800000, v27\n" \
  "v_fmamk_f32 v199, v6, 0x41880000, v27\n" \
  "v_fmamk_f32 v201, v6, 0x41900000, v27\n" \
  "v_fmamk_f32 v203, v6, 0x41980000, v27\n" \
  "v_cos_f32 v152, v31\n" \
  "v_fmamk_f32 v161, v26, 0x3f800000, v31\n" \
  "v_fmamk_f32 v162, v26, 0x40000000, v31\n" \
  "v_fmamk_f32 v163, v26, 0x40400000, v31\n" \
  "v_sin_f32 v189, v27\n" \
  "v_cos_f32 v190, v191\n" \
  "v_cos_f32 v192, v193\n" \
  "v_cos_f32 v194, v195\n" \
  "v_cos_f32 v196, v197\n" \
  "v_cos_f32 v198, v199\n" \
  "v_cos_f32 v200, v201\n" \
  "v_cos_f32 v202, v203\n" \
  "v_sin_f32 v160, v31\n" \
  "v_cos_f32 v153, v161\n" \
  "v_cos_f32 v154, v162\n" \
  "v_cos_f32 v155, v163\n" \
  "v_sin_f32 v191, v191\n" \
  "v_sin_f32 v193, v193\n" \
  "v_sin_f32 v195, v195\n" \
  "v_sin_f32 v197, v197\n" \
  "v_sin_f32 v199, v199\n" \
  "v_sin_f32 v201, v201\n" \
  "v_sin_f32 v203, v203\n" \
  "v_sin_f32 v161, v161\n" \
  "v_sin_f32 v162, v162\n" \
  "v_sin_f32 v163, v163\n" \
  "v_cvt_pk_bf16_f32 v168, v188, v190\n" \
  "v_cvt_pk_bf16_f32 v176, v189, v191\n" \
  "v_cvt_pk_bf16_f32 v184, -v189, -v191\n" \
  "v_cvt_pk_bf16_f32 v169, v192, v194\n" \
  "v_cvt_pk_bf16_f32 v177, v193, v195\n" \
  "v_cvt_pk_bf16_f32 v185, -v193, -v195\n" \
  "v_cvt_pk_bf16_f32 v170, v196, v198\n" \
  "v_cvt_pk_bf16_f32 v178, v197, v199\n" \
  "v_cvt_pk_bf16_f32 v186, -v197, -v199\n" \
  "v_cvt_pk_bf16_f32 v171, v200, v202\n" \
  "v_cvt_pk_bf16_f32 v179, v201, v203\n" \
  "v_cvt_pk_bf16_f32 v187, -v201, -v203\n" \
  "v_add_f32 v19, 0x41800000, v19\n" \
  "v_mul_f32 v6, 0x3d000000, v19\n" \
  "v_mul_f32 v27, v20, v6\n" \
  "v_mul_f32 v26, 0x3a000000, v19\n" \
  "v_mul_f32 v31, v22, v26\n" \
  "v_cos_f32 v188, v27\n" \
  "v_fmamk_f32 v191, v6, 0x3f800000, v27\n" \
  "v_fmamk_f32 v193, v6, 0x40000000, v27\n" \
  "v_fmamk_f32 v195, v6, 0x40400000, v27\n" \
  "v_fmamk_f32 v197, v6, 0x41800000, v27\n" \
  "v_fmamk_f32 v199, v6, 0x41880000, v27\n" \
  "v_fmamk_f32 v201, v6, 0x41900000, v27\n" \
  "v_fmamk_f32 v203, v6, 0x41980000, v27\n" \
  "v_cos_f32 v156, v31\n" \
  "v_fmamk_f32 v165, v26, 0x3f800000, v31\n" \
  "v_fmamk_f32 v166, v26, 0x40000000, v31\n" \
  "v_fmamk_f32 v167, v26, 0x40400000, v31\n" \
  "v_sin_f32 v189, v27\n" \
  "v_cos_f32 v190, v191\n" \
  "v_cos_f32 v192, v193\n" \
  "v_cos_f32 v194, v195\n" \
  "v_cos_f32 v196, v197\n" \
  "v_cos_f32 v198, v199\n" \
  "v_cos_f32 v200, v201\n" \
  "v_cos_f32 v202, v203\n" \
  "v_sin_f32 v164, v31\n" \
  "v_cos_f32 v157, v165\n" \
  "v_cos_f32 v158, v166\n" \
  "v_cos_f32 v159, v167\n" \
  "v_sin_f32 v191, v191\n" \
  "v_sin_f32 v193, v193\n" \
  "v_sin_f32 v195, v195\n" \
  "v_sin_f32 v197, v197\n" \
  "v_sin_f32 v199, v199\n" \
  "v_sin_f32 v201, v201\n" \
  "v_sin_f32 v203, v203\n" \
  "v_sin_f32 v165, v165\n" \
  "v_sin_f32 v166, v166\n" \
  "v_sin_f32 v167, v167\n" \
  "v_cvt_pk_bf16_f32 v172, v188, v190\n" \
  "v_cvt_pk_bf16_f32 v180, v189, v191\n" \
  "v_cvt_pk_bf16_f32 v228, -v189, -v191\n" \
  "v_cvt_pk_bf16_f32 v173, v192, v194\n" \
  "v_cvt_pk_bf16_f32 v181, v193, v195\n" \
  "v_cvt_pk_bf16_f32 v229, -v193, -v195\n" \
  "v_cvt_pk_bf16_f32 v174, v196, v198\n" \
  "v_cvt_pk_bf16_f32 v182, v197, v199\n" \
  "v_cvt_pk_bf16_f32 v230, -v197, -v199\n" \
  "v_cvt_pk_bf16_f32 v175, v200, v202\n" \
  "v_cvt_pk_bf16_f32 v183, v201, v203\n" \
  "v_cvt_pk_bf16_f32 v231, -v201, -v203\n" \
  "s_mov_b32 s69, 0x3a000000\n" \
  "v_pk_mul_f32 v[68:69], v[68:69], s[68:69] op_sel:[0,1] op_sel_hi:[1,1]\n" \
  "v_pk_mul_f32 v[76:77], v[76:77], s[68:69] op_sel:[0,1] op_sel_hi:[1,1]\n" \
  "v_pk_mul_f32 v[70:71], v[70:71], s[68:69] op_sel:[0,1] op_sel_hi:[1,1]\n" \
  "v_pk_mul_f32 v[78:79], v[78:79], s[68:69] op_sel:[0,1] op_sel_hi:[1,1]\n" \
  "v_pk_mul_f32 v[72:73], v[72:73], s[68:69] op_sel:[0,1] op_sel_hi:[1,1]\n" \
  "v_pk_mul_f32 v[80:81], v[80:81], s[68:69] op_sel:[0,1] op_sel_hi:[1,1]\n" \
  "v_pk_mul_f32 v[74:75], v[74:75], s[68:69] op_sel:[0,1] op_sel_hi:[1,1]\n" \
  "v_pk_mul_f32 v[82:83], v[82:83], s[68:69] op_sel:[0,1] op_sel_hi:[1,1]\n" \
  "s_lshr_b32 s12, s74, 0x1\n" \
  "v_and_b32 v2, 3, v1\n" \
  "v_bfe_u32 v3, v1, 2, 1\n" \
  "v_add_u32 v2, s12, v2\n" \
  "v_min_u32 v2, 0x6f, v2\n" \
  "v_mul_u32_u24 v3, 0x70, v3\n" \
  "v_add_u32 v2, v2, v3\n" \
  "v_mul_u32_u24 v3, 0x925, v2\n" \
  "v_lshrrev_b32 v3, 16, v3\n" \
  "v_mul_u32_u24 v4, 28, v3\n" \
  "v_sub_u32 v2, v2, v4\n" \
  "v_lshl_or_b32 v2, v2, 3, v3\n" \
  "v_and_b32 v3, 7, v1\n" \
  "s_lshl_b32 s12, s3, 0x3\n" \
  "v_add_u32 v3, s12, v3\n" \
  "v_lshl_add_u32 v3, v3, 3, s73\n" \
  "v_and_b32 v4, 0, v1\n" \
  "v_cmp_eq_u32 vcc, 0, v4\n" \
  "s_nop 1\n" \
  "v_cndmask_b32 v2, v3, v2, vcc\n" \
  "v_bfe_u32 v3, v1, 4, 1\n" \
  "v_lshl_or_b32 v2, v2, 1, v3\n" \
  "v_lshlrev_b32 v13, 6, v2\n" \
  "v_add_u32 v13, 0x1a01000, v13\n" \
  "v_lshrrev_b32 v2, 3, v17\n" \
  "v_mul_u32_u24 v30, 0x1c0000, v2\n" \
  "v_lshl_or_b32 v30, v18, 8, v30\n" \
  "v_and_b32 v2, 7, v17\n" \
  "v_lshl_or_b32 v30, v2, 4, v30\n" \
  "v_lshrrev_b32 v2, 1, v18\n" \
  "v_mul_u32_u24 v87, 0xe000, v2\n" \
  "v_and_b32 v2, 31, v1\n" \
  "v_lshl_add_u32 v87, v2, 4, v87\n" \
  "v_and_b32 v2, 1, v18\n" \
  "v_lshl_or_b32 v102, v2, 31, v87\n" \
  "s_lshl_b32 s12, s70, 0x2\n" \
  "s_add_u32 s12, s12, s3\n" \
  "s_mul_i32 s50, s12, 0x38000\n" \
  "s_lshl_b32 s12, s74, 0x8\n" \
  "s_add_u32 s50, s50, s12\n" \
  "s_add_u32 s50, s50, 0x1000000\n" \
  "s_cmp_lt_u32 s3, 2\n" \
  "s_cbranch_scc1 LF_e%=\n" \
  "s_waitcnt vmcnt(0)\n" \
  "s_lshl_b32 s12, s2, 0x1\n" \
  "s_add_u32 s12, s12, s3\n" \
  "s_lshl_b32 s12, s12, 0x6\n" \
  "s_add_u32 s12, s12, 0x1a00f80\n" \
  "buffer_store_dwordx2 v[10:11], v86, s[36:39], s12 offen sc1\n" \
  "buffer_load_dwordx4 v[244:247], v96, s[36:39], s20 offen sc1\n" \
  "buffer_load_dwordx4 v[248:251], v96, s[36:39], s21 offen sc1\n" \
  "buffer_load_dwordx4 v[252:255], v96, s[36:39], s22 offen sc1\n" \
  "buffer_load_dwordx4 v[104:107], v96, s[36:39], s23 offen sc1\n" \
  "buffer_load_dwordx4 v[108:111], v96, s[36:39], s24 offen sc1\n" \
  "buffer_load_dwordx4 v[112:115], v96, s[36:39], s25 offen sc1\n" \
  "buffer_load_dwordx4 v[64:67], v96, s[36:39], s26 offen sc1\n" \
  "buffer_load_dwordx4 v[32:35], v96, s[36:39], s27 offen sc1\n" \
  "LF_e%=:\n" \
  "s_cmp_lt_u32 s3, 2\n" \
  "s_cbranch_scc0 LV_b%=\n" \
  "s_waitcnt vmcnt(0)\n" \
  "v_xor_b32 v2, s48, v247\n" \
  "v_xor_b32 v3, s67, v246\n" \
  "v_bitop3_b32 v2, v2, v251, s48 bitop3:0xf6\n" \
  "v_bitop3_b32 v3, v3, v250, s67 bitop3:0xf6\n" \
  "v_bitop3_b32 v2, v2, v255, s48 bitop3:0xf6\n" \
  "v_bitop3_b32 v3, v3, v254, s67 bitop3:0xf6\n" \
  "v_bitop3_b32 v2, v2, v107, s48 bitop3:0xf6\n" \
  "v_bitop3_b32 v3, v3, v106, s67 bitop3:0xf6\n" \
  "v_bitop3_b32 v2, v2, v111, s48 bitop3:0xf6\n" \
  "v_bitop3_b32 v3, v3, v110, s67 bitop3:0xf6\n" \
  "v_bitop3_b32 v2, v2, v115, s48 bitop3:0xf6\n" \
  "v_bitop3_b32 v3, v3, v114, s67 bitop3:0xf6\n" \
  "v_bitop3_b32 v2, v2, v67, s48 bitop3:0xf6\n" \
  "v_bitop3_b32 v3, v3, v66, s67 bitop3:0xf6\n" \
  "v_bitop3_b32 v2, v2, v35, s48 bitop3:0xf6\n" \
  "v_bitop3_b32 v3, v3, v34, s67 bitop3:0xf6\n" \
  "v_lshrrev_b32 v3, 16, v3\n" \
  "v_or_b32 v2, v2, v3\n" \
  "v_cmp_eq_u32 vcc, 0, v2\n" \
  "s_nop 0\n" \
  "s_cmp_eq_u64 vcc, -1\n" \
  "s_cbranch_scc0 LX_slow%=\n" \
  "LX_ok%=:\n" \
  "ds_write_b64 v97, v[244:245]\n" \
  "ds_write_b64 v97, v[248:249] offset:64\n" \
  "ds_write_b64 v97, v[252:253] offset:128\n" \
  "ds_write_b64 v97, v[104:105] offset:192\n" \
  "ds_write_b64 v97, v[108:109] offset:256\n" \
  "ds_write_b64 v97, v[112:113] offset:320\n" \
  "ds_write_b64 v97, v[64:65] offset:384\n" \
  "ds_write_b64 v97, v[32:33] offset:448\n" \
  "LV_b%=:\n" \
  "s_waitcnt lgkmcnt(0)\n" \
  "s_barrier\n" \
  "ds_read_b128 a[192:195], v100\n" \
  "ds_read_b128 a[196:199], v100 offset:2048\n" \
  "ds_read_b128 a[200:203], v100 offset:4096\n" \
  "ds_read_b128 a[204:207], v100 offset:6144\n" \
  "ds_read_b128 a[208:211], v100 offset:256\n" \
  "ds_read_b128 a[212:215], v100 offset:2304\n" \
  "ds_read_b128 a[216:219], v100 offset:4352\n" \
  "ds_read_b128 a[220:223], v100 offset:6400\n" \
  "s_cmp_eq_u32 s2, 0\n" \
  "s_cselect_b32 s60, 0x0, 0x80000000\n" \
  "v_or_b32 v7, s60, v12\n" \
  "s_mov_b32 s60, 0x1a00000\n" \
  "buffer_store_dword v10, v7, s[36:39], s60 offen sc1\n" \
  "s_waitcnt lgkmcnt(7)\n" \
  "v_mfma_f32_16x16x32_f16 v[232:235], a[192:195], v[36:39], 0\n" \
  "v_mfma_f32_16x16x32_f16 v[236:239], a[192:195], v[52:55], 0\n" \
  "s_waitcnt lgkmcnt(6)\n" \
  "v_mfma_f32_16x16x32_f16 v[232:235], a[196:199], v[40:43], v[232:235]\n" \
  "v_mfma_f32_16x16x32_f16 v[236:239], a[196:199], v[56:59], v[236:239]\n" \
  "s_waitcnt lgkmcnt(5)\n" \
  "v_mfma_f32_16x16x32_f16 v[232:235], a[200:203], v[44:47], v[232:235]\n" \
  "v_mfma_f32_16x16x32_f16 v[236:239], a[200:203], v[60:63], v[236:239]\n" \
  "s_waitcnt lgkmcnt(4)\n" \
  "v_mfma_f32_16x16x32_f16 v[232:235], a[204:207], v[48:51], v[232:235]\n" \
  "v_mfma_f32_16x16x32_f16 v[236:239], a[204:207], v[116:119], v[236:239]\n" \
  "s_waitcnt lgkmcnt(3)\n" \
  "v_mfma_f32_16x16x32_f16 v[240:243], a[208:211], v[36:39], 0\n" \
  "v_mfma_f32_16x16x32_f16 v[220:223], a[208:211], v[52:55], 0\n" \
  "s_waitcnt lgkmcnt(2)\n" \
  "v_mfma_f32_16x16x32_f16 v[240:243], a[212:215], v[40:43], v[240:243]\n" \
  "s_nop 2\n" \
  "v_pk_mul_f32 v[188:189], v[236:237], v[76:77]\n" \
  "v_pk_mul_f32 v[192:193], v[238:239], v[78:79]\n" \
  "s_nop 1\n" \
  "v_mfma_f32_16x16x32_f16 v[220:223], a[212:215], v[56:59], v[220:223]\n" \
  "v_pk_mul_f32 v[190:191], v[232:233], v[76:77]\n" \
  "v_pk_mul_f32 v[194:195], v[234:235], v[78:79]\n" \
  "s_waitcnt lgkmcnt(1)\n" \
  "s_nop 0\n" \
  "v_mfma_f32_16x16x32_f16 v[240:243], a[216:219], v[44:47], v[240:243]\n" \
  "v_pk_fma_f32 v[188:189], v[232:233], v[68:69], v[188:189]\n" \
  "v_pk_fma_f32 v[192:193], v[234:235], v[70:71], v[192:193]\n" \
  "s_nop 1\n" \
  "v_mfma_f32_16x16x32_f16 v[220:223], a[216:219], v[60:63], v[220:223]\n" \
  "v_pk_fma_f32 v[190:191], v[236:237], v[68:69], v[190:191] neg_lo:[0,0,1] neg_hi:[0,0,1]\n" \
  "v_pk_fma_f32 v[194:195], v[238:239], v[70:71], v[194:195] neg_lo:[0,0,1] neg_hi:[0,0,1]\n" \
  "s_waitcnt lgkmcnt(0)\n" \
  "s_nop 0\n" \
  "v_mfma_f32_16x16x32_f16 v[240:243], a[220:223], v[48:51], v[240:243]\n" \
  "v_cvt_pk_bf16_f32 v204, v188, v189\n" \
  "v_cvt_pk_bf16_f32 v205, v192, v193\n" \
  "v_mfma_f32_16x16x32_f16 v[220:223], a[220:223], v[116:119], v[220:223]\n" \
  "v_cvt_pk_bf16_f32 v208, v190, v191\n" \
  "v_cvt_pk_bf16_f32 v209, v194, v195\n" \
  "s_nop 5\n" \
  "v_pk_mul_f32 v[196:197], v[220:221], v[80:81]\n" \
  "v_pk_mul_f32 v[200:201], v[222:223], v[82:83]\n" \
  "v_pk_mul_f32 v[198:199], v[240:241], v[80:81]\n" \
  "v_pk_mul_f32 v[202:203], v[242:243], v[82:83]\n" \
  "v_pk_fma_f32 v[196:197], v[240:241], v[72:73], v[196:197]\n" \
  "v_pk_fma_f32 v[200:201], v[242:243], v[74:75], v[200:201]\n" \
  "v_pk_fma_f32 v[198:199], v[220:221], v[72:73], v[198:199] neg_lo:[0,0,1] neg_hi:[0,0,1]\n" \
  "v_pk_fma_f32 v[202:203], v[222:223], v[74:75], v[202:203] neg_lo:[0,0,1] neg_hi:[0,0,1]\n" \
  "v_cvt_pk_bf16_f32 v206, v196, v197\n" \
  "v_cvt_pk_bf16_f32 v207, v200, v201\n" \
  "v_cvt_pk_bf16_f32 v210, v198, v199\n" \
  "v_cvt_pk_bf16_f32 v211, v202, v203\n" \
  "v_mfma_f32_16x16x32_bf16 v[232:235], v[168:171], v[204:207], 0\n" \
  "s_nop 0\n" \
  "v_mfma_f32_16x16x32_bf16 v[236:239], v[168:171], v[208:211], 0\n" \
  "v_mfma_f32_16x16x32_bf16 v[232:235], v[176:179], v[208:211], v[232:235]\n" \
  "v_mfma_f32_16x16x32_bf16 v[236:239], v[184:187], v[204:207], v[236:239]\n" \
  "v_mfma_f32_16x16x32_bf16 v[240:243], v[172:175], v[204:207], 0\n" \
  "v_mfma_f32_16x16x32_bf16 v[220:223], v[172:175], v[208:211], 0\n" \
  "v_mfma_f32_16x16x32_bf16 v[240:243], v[180:183], v[208:211], v[240:243]\n" \
  "v_mfma_f32_16x16x32_bf16 v[220:223], v[228:231], v[204:207], v[220:223]\n" \
  "s_cmp_lt_u32 s3, 2\n" \
  "s_cbranch_scc0 LU_b%=\n" \
  "ds_write_b16 v98, v246\n" \
  "ds_write_b16 v98, v250 offset:512\n" \
  "ds_write_b16 v98, v254 offset:1024\n" \
  "ds_write_b16 v98, v106 offset:1536\n" \
  "ds_write_b16 v98, v110 offset:2048\n" \
  "ds_write_b16 v98, v114 offset:2560\n" \
  "ds_write_b16 v98, v66 offset:3072\n" \
  "ds_write_b16 v98, v34 offset:3584\n" \
  "s_branch LU_e%=\n" \
  "LU_b%=:\n" \
  "s_waitcnt vmcnt(1)\n" \
  "v_xor_b32 v2, s48, v247\n" \
  "v_xor_b32 v3, s67, v246\n" \
  "v_bitop3_b32 v2, v2, v251, s48 bitop3:0xf6\n" \
  "v_bitop3_b32 v3, v3, v250, s67 bitop3:0xf6\n" \
  "v_bitop3_b32 v2, v2, v255, s48 bitop3:0xf6\n" \
  "v_bitop3_b32 v3, v3, v254, s67 bitop3:0xf6\n" \
  "v_bitop3_b32 v2, v2, v107, s48 bitop3:0xf6\n" \
  "v_bitop3_b32 v3, v3, v106, s67 bitop3:0xf6\n" \
  "v_bitop3_b32 v2, v2, v111, s48 bitop3:0xf6\n" \
  "v_bitop3_b32 v3, v3, v110, s67 bitop3:0xf6\n" \
  "v_bitop3_b32 v2, v2, v115, s48 bitop3:0xf6\n" \
  "v_bitop3_b32 v3, v3, v114, s67 bitop3:0xf6\n" \
  "v_bitop3_b32 v2, v2, v67, s48 bitop3:0xf6\n" \
  "v_bitop3_b32 v3, v3, v66, s67 bitop3:0xf6\n" \
  "v_bitop3_b32 v2, v2, v35, s48 bitop3:0xf6\n" \
  "v_bitop3_b32 v3, v3, v34, s67 bitop3:0xf6\n" \
  "v_lshrrev_b32 v3, 16, v3\n" \
  "v_or_b32 v2, v2, v3\n" \
  "v_cmp_eq_u32 vcc, 0, v2\n" \
  "s_nop 0\n" \
  "s_cmp_eq_u64 vcc, -1\n" \
  "s_cbranch_scc0 LW_slow%=\n" \
  "LW_ok%=:\n" \
  "ds_write_b64 v99, v[244:245]\n" \
  "ds_write_b64 v99, v[248:249] offset:1024\n" \
  "ds_write_b64 v99, v[252:253] offset:2048\n" \
  "ds_write_b64 v99, v[104:105] offset:3072\n" \
  "ds_write_b64 v99, v[108:109] offset:4096\n" \
  "ds_write_b64 v99, v[112:113] offset:5120\n" \
  "ds_write_b64 v99, v[64:65] offset:6144\n" \
  "ds_write_b64 v99, v[32:33] offset:7168\n" \
  "ds_write_b16 v98, v246\n" \
  "ds_write_b16 v98, v250 offset:512\n" \
  "ds_write_b16 v98, v254 offset:1024\n" \
  "ds_write_b16 v98, v106 offset:1536\n" \
  "ds_write_b16 v98, v110 offset:2048\n" \
  "ds_write_b16 v98, v114 offset:2560\n" \
  "ds_write_b16 v98, v66 offset:3072\n" \
  "ds_write_b16 v98, v34 offset:3584\n" \
  "LU_e%=:\n" \
  "s_waitcnt lgkmcnt(0)\n" \
  "s_barrier\n" \
  "ds_read_b128 v[244:247], v25\n" \
  "ds_read_b128 v[248:251], v25 offset:16\n" \
  "ds_read_b128 v[252:255], v28\n" \
  "ds_read_b128 v[104:107], v28 offset:256\n" \
  "ds_read_b64 v[108:109], v29\n" \
  "ds_read_b64 v[110:111], v29 offset:2048\n" \
  "s_waitcnt lgkmcnt(4)\n" \
  "v_lshlrev_b32 v68, 16, v244\n" \
  "v_and_b32 v76, 0xffff0000, v244\n" \
  "v_lshlrev_b32 v69, 16, v245\n" \
  "v_and_b32 v77, 0xffff0000, v245\n" \
  "v_lshlrev_b32 v70, 16, v246\n" \
  "v_and_b32 v78, 0xffff0000, v246\n" \
  "v_lshlrev_b32 v71, 16, v247\n" \
  "v_and_b32 v79, 0xffff0000, v247\n" \
  "v_lshlrev_b32 v72, 16, v248\n" \
  "v_and_b32 v80, 0xffff0000, v248\n" \
  "v_lshlrev_b32 v73, 16, v249\n" \
  "v_and_b32 v81, 0xffff0000, v249\n" \
  "v_lshlrev_b32 v74, 16, v250\n" \
  "v_and_b32 v82, 0xffff0000, v250\n" \
  "v_lshlrev_b32 v75, 16, v251\n" \
  "v_and_b32 v83, 0xffff0000, v251\n" \
  "v_pk_mul_f32 v[188:189], v[236:237], v[76:77]\n" \
  "v_pk_mul_f32 v[192:193], v[238:239], v[78:79]\n" \
  "v_pk_mul_f32 v[190:191], v[232:233], v[76:77]\n" \
  "v_pk_mul_f32 v[194:195], v[234:235], v[78:79]\n" \
  "v_pk_fma_f32 v[188:189], v[232:233], v[68:69], v[188:189] neg_lo:[0,0,1] neg_hi:[0,0,1]\n" \
  "v_pk_fma_f32 v[192:193], v[234:235], v[70:71], v[192:193] neg_lo:[0,0,1] neg_hi:[0,0,1]\n" \
  "v_pk_fma_f32 v[190:191], v[236:237], v[68:69], v[190:191]\n" \
  "v_pk_fma_f32 v[194:195], v[238:239], v[70:71], v[194:195]\n" \
  "v_cvt_pk_bf16_f32 v212, v188, v189\n" \
  "v_cvt_pk_bf16_f32 v213, v192, v193\n" \
  "v_cvt_pk_bf16_f32 v216, v190, v191\n" \
  "v_cvt_pk_bf16_f32 v217, v194, v195\n" \
  "v_pk_mul_f32 v[196:197], v[220:221], v[80:81]\n" \
  "v_pk_mul_f32 v[200:201], v[222:223], v[82:83]\n" \
  "v_pk_mul_f32 v[198:199], v[240:241], v[80:81]\n" \
  "v_pk_mul_f32 v[202:203], v[242:243], v[82:83]\n" \
  "v_pk_fma_f32 v[196:197], v[240:241], v[72:73], v[196:197] neg_lo:[0,0,1] neg_hi:[0,0,1]\n" \
  "v_pk_fma_f32 v[200:201], v[242:243], v[74:75], v[200:201] neg_lo:[0,0,1] neg_hi:[0,0,1]\n" \
  "v_pk_fma_f32 v[198:199], v[220:221], v[72:73], v[198:199]\n" \
  "v_pk_fma_f32 v[202:203], v[222:223], v[74:75], v[202:203]\n" \
  "v_cvt_pk_bf16_f32 v214, v196, v197\n" \
  "v_cvt_pk_bf16_f32 v215, v200, v201\n" \
  "v_cvt_pk_bf16_f32 v218, v198, v199\n" \
  "v_cvt_pk_bf16_f32 v219, v202, v203\n" \
  "v_mfma_f32_16x16x32_bf16 v[232:235], v[212:215], v[168:171], 0\n" \
  "s_waitcnt lgkmcnt(0)\n" \
  "v_lshlrev_b32 v36, 16, v108\n" \
  "v_mfma_f32_16x16x32_bf16 v[236:239], v[212:215], v[176:179], 0\n" \
  "v_and_b32 v37, 0xffff0000, v108\n" \
  "v_lshlrev_b32 v38, 16, v109\n" \
  "v_mfma_f32_16x16x32_bf16 v[232:235], v[216:219], v[184:187], v[232:235]\n" \
  "v_and_b32 v39, 0xffff0000, v109\n" \
  "v_lshlrev_b32 v40, 16, v110\n" \
  "v_mfma_f32_16x16x32_bf16 v[236:239], v[216:219], v[168:171], v[236:239]\n" \
  "v_and_b32 v41, 0xffff0000, v110\n" \
  "v_lshlrev_b32 v42, 16, v111\n" \
  "v_mfma_f32_16x16x32_bf16 v[240:243], v[212:215], v[172:175], 0\n" \
  "v_and_b32 v43, 0xffff0000, v111\n" \
  "v_mfma_f32_16x16x32_bf16 v[220:223], v[212:215], v[180:183], 0\n" \
  "v_mfma_f32_16x16x32_bf16 v[240:243], v[216:219], v[228:231], v[240:243]\n" \
  "v_mfma_f32_16x16x32_bf16 v[220:223], v[216:219], v[172:175], v[220:223]\n" \
  "s_nop 0\n" \
  "v_pk_mul_f32 v[188:189], v[236:237], v[160:161]\n" \
  "v_pk_mul_f32 v[192:193], v[238:239], v[162:163]\n" \
  "v_pk_mul_f32 v[190:191], v[232:233], v[160:161]\n" \
  "v_pk_mul_f32 v[194:195], v[234:235], v[162:163]\n" \
  "v_pk_fma_f32 v[188:189], v[232:233], v[152:153], v[188:189] neg_lo:[0,0,1] neg_hi:[0,0,1]\n" \
  "v_pk_fma_f32 v[192:193], v[234:235], v[154:155], v[192:193] neg_lo:[0,0,1] neg_hi:[0,0,1]\n" \
  "v_pk_fma_f32 v[190:191], v[236:237], v[152:153], v[190:191]\n" \
  "v_pk_fma_f32 v[194:195], v[238:239], v[154:155], v[194:195]\n" \
  "v_cvt_pk_bf16_f32 v113, v188, v189\n" \
  "v_cvt_pk_bf16_f32 v115, v192, v193\n" \
  "v_cvt_pk_bf16_f32 v112, v190, v191\n" \
  "v_cvt_pk_bf16_f32 v114, v194, v195\n" \
  "ds_write_b128 v103, v[112:115]\n" \
  "v_pk_mul_f32 v[196:197], v[220:221], v[164:165]\n" \
  "v_pk_mul_f32 v[200:201], v[222:223], v[166:167]\n" \
  "v_pk_mul_f32 v[198:199], v[240:241], v[164:165]\n" \
  "v_pk_mul_f32 v[202:203], v[242:243], v[166:167]\n" \
  "v_pk_fma_f32 v[196:197], v[240:241], v[156:157], v[196:197] neg_lo:[0,0,1] neg_hi:[0,0,1]\n" \
  "v_pk_fma_f32 v[200:201], v[242:243], v[158:159], v[200:201] neg_lo:[0,0,1] neg_hi:[0,0,1]\n" \
  "v_pk_fma_f32 v[198:199], v[220:221], v[156:157], v[198:199]\n" \
  "v_pk_fma_f32 v[202:203], v[222:223], v[158:159], v[202:203]\n" \
  "v_cvt_pk_bf16_f32 v65, v196, v197\n" \
  "v_cvt_pk_bf16_f32 v67, v200, v201\n" \
  "v_cvt_pk_bf16_f32 v64, v198, v199\n" \
  "v_cvt_pk_bf16_f32 v66, v202, v203\n" \
  "ds_write_b128 v103, v[64:67] offset:1024\n" \
  "s_waitcnt lgkmcnt(0)\n" \
  "s_barrier\n" \
  "ds_read_b128 a[192:195], v101\n" \
  "ds_read_b128 a[196:199], v101 offset:2048\n" \
  "ds_read_b128 a[200:203], v101 offset:4096\n" \
  "ds_read_b128 a[204:207], v101 offset:6144\n" \
  "ds_read_b128 a[208:211], v101 offset:1024\n" \
  "ds_read_b128 a[212:215], v101 offset:3072\n" \
  "ds_read_b128 a[216:219], v101 offset:5120\n" \
  "ds_read_b128 a[220:223], v101 offset:7168\n" \
  "s_cmp_lt_u32 s3, 2\n" \
  "s_cbranch_scc1 LD_e%=\n" \
  "buffer_store_dwordx4 a[176:179], v95, s[36:39], s75 offen sc1\n" \
  "buffer_store_dwordx4 a[180:183], v95, s[36:39], s75 offen offset:1024 sc1\n" \
  "buffer_store_dwordx4 a[184:187], v95, s[36:39], s75 offen offset:2048 sc1\n" \
  "buffer_store_dwordx4 a[188:191], v9, s[36:39], s75 offen offset:3072 sc1\n" \
  "LD_e%=:\n" \
  "buffer_load_dwordx2 v[26:27], v13, s[36:39], 0 offen sc1\n" \
  "s_lshl_b32 s60, s66, 0xd\n" \
  "s_add_u32 s63, s60, 0xa00000\n" \
  "s_add_u32 s60, s60, 0x800000\n" \
  "s_waitcnt lgkmcnt(7)\n" \
  "v_mfma_f32_16x16x32_bf16 v[236:239], v[124:127], a[192:195], 0\n" \
  "v_mfma_f32_16x16x32_bf16 v[232:235], v[136:139], a[192:195], 0\n" \
  "s_waitcnt lgkmcnt(6)\n" \
  "v_mfma_f32_16x16x32_bf16 v[236:239], v[120:123], a[196:199], v[236:239]\n" \
  "v_mfma_f32_16x16x32_bf16 v[232:235], v[140:143], a[196:199], v[232:235]\n" \
  "s_waitcnt lgkmcnt(5)\n" \
  "v_mfma_f32_16x16x32_bf16 v[236:239], v[128:131], a[200:203], v[236:239]\n" \
  "v_mfma_f32_16x16x32_bf16 v[232:235], v[144:147], a[200:203], v[232:235]\n" \
  "s_waitcnt lgkmcnt(4)\n" \
  "v_mfma_f32_16x16x32_bf16 v[236:239], v[132:135], a[204:207], v[236:239]\n" \
  "v_mfma_f32_16x16x32_bf16 v[232:235], v[148:151], a[204:207], v[232:235]\n" \
  "s_waitcnt lgkmcnt(3)\n" \
  "v_mfma_f32_16x16x32_bf16 v[220:223], v[124:127], a[208:211], 0\n" \
  "v_mfma_f32_16x16x32_bf16 v[240:243], v[136:139], a[208:211], 0\n" \
  "s_waitcnt lgkmcnt(2)\n" \
  "v_mfma_f32_16x16x32_bf16 v[220:223], v[120:123], a[212:215], v[220:223]\n" \
  "s_nop 2\n" \
  "v_fma_mix_f32 v188, v252, v36, v232 op_sel:[0,0,0] op_sel_hi:[1,0,0]\n" \
  "v_fma_mix_f32 v192, v253, v36, v236 op_sel:[0,0,0] op_sel_hi:[1,0,0]\n" \
  "v_mfma_f32_16x16x32_bf16 v[240:243], v[140:143], a[212:215], v[240:243]\n" \
  "v_fma_mix_f32 v189, v252, v37, v233 op_sel:[1,0,0] op_sel_hi:[1,0,0]\n" \
  "v_fma_mix_f32 v193, v253, v37, v237 op_sel:[1,0,0] op_sel_hi:[1,0,0]\n" \
  "s_waitcnt lgkmcnt(1)\n" \
  "v_mfma_f32_16x16x32_bf16 v[220:223], v[128:131], a[216:219], v[220:223]\n" \
  "v_fma_mix_f32 v190, v254, v38, v234 op_sel:[0,0,0] op_sel_hi:[1,0,0]\n" \
  "v_fma_mix_f32 v194, v255, v38, v238 op_sel:[0,0,0] op_sel_hi:[1,0,0]\n" \
  "v_mfma_f32_16x16x32_bf16 v[240:243], v[144:147], a[216:219], v[240:243]\n" \
  "v_fma_mix_f32 v191, v254, v39, v235 op_sel:[1,0,0] op_sel_hi:[1,0,0]\n" \
  "v_fma_mix_f32 v195, v255, v39, v239 op_sel:[1,0,0] op_sel_hi:[1,0,0]\n" \
  "s_waitcnt lgkmcnt(0)\n" \
  "v_mfma_f32_16x16x32_bf16 v[220:223], v[132:135], a[220:223], v[220:223]\n" \
  "v_cvt_pk_bf16_f32 v112, v188, v192\n" \
  "v_cvt_pk_bf16_f32 v113, v189, v193\n" \
  "v_mfma_f32_16x16x32_bf16 v[240:243], v[148:151], a[220:223], v[240:243]\n" \
  "v_cvt_pk_bf16_f32 v114, v190, v194\n" \
  "v_cvt_pk_bf16_f32 v115, v191, v195\n" \
  "v_and_b32 v112, s76, v112\n" \
  "v_and_b32 v113, s76, v113\n" \
  "v_and_b32 v114, s76, v114\n" \
  "v_and_b32 v115, s76, v115\n" \
  "v_min_u32 v112, -2, v112\n" \
  "buffer_store_dwordx4 v[112:115], v24, s[36:39], s60 offen sc1\n" \
  "v_fma_mix_f32 v196, v104, v40, v240 op_sel:[0,0,0] op_sel_hi:[1,0,0]\n" \
  "v_fma_mix_f32 v200, v105, v40, v220 op_sel:[0,0,0] op_sel_hi:[1,0,0]\n" \
  "v_fma_mix_f32 v197, v104, v41, v241 op_sel:[1,0,0] op_sel_hi:[1,0,0]\n" \
  "v_fma_mix_f32 v201, v105, v41, v221 op_sel:[1,0,0] op_sel_hi:[1,0,0]\n" \
  "v_fma_mix_f32 v198, v106, v42, v242 op_sel:[0,0,0] op_sel_hi:[1,0,0]\n" \
  "v_fma_mix_f32 v202, v107, v42, v222 op_sel:[0,0,0] op_sel_hi:[1,0,0]\n" \
  "v_fma_mix_f32 v199, v106, v43, v243 op_sel:[1,0,0] op_sel_hi:[1,0,0]\n" \
  "v_fma_mix_f32 v203, v107, v43, v223 op_sel:[1,0,0] op_sel_hi:[1,0,0]\n" \
  "v_cvt_pk_bf16_f32 v64, v196, v200\n" \
  "v_cvt_pk_bf16_f32 v65, v197, v201\n" \
  "v_cvt_pk_bf16_f32 v66, v198, v202\n" \
  "v_cvt_pk_bf16_f32 v67, v199, v203\n" \
  "v_and_b32 v64, s76, v64\n" \
  "v_and_b32 v65, s76, v65\n" \
  "v_and_b32 v66, s76, v66\n" \
  "v_and_b32 v67, s76, v67\n" \
  "v_min_u32 v64, -2, v64\n" \
  "buffer_store_dwordx4 v[64:67], v24, s[36:39], s60 offen offset:256 sc1\n" \
  "s_mov_b32 s12, 0x3d800000\n" \
  "s_mov_b32 s77, s63\n" \
  "v_pk_mul_f32 v[232:233], v[232:233], s[12:13] op_sel_hi:[1,0]\n" \
  "v_pk_mul_f32 v[234:235], v[234:235], s[12:13] op_sel_hi:[1,0]\n" \
  "v_pk_mul_f32 v[236:237], v[236:237], s[12:13] op_sel_hi:[1,0]\n" \
  "v_pk_mul_f32 v[238:239], v[238:239], s[12:13] op_sel_hi:[1,0]\n" \
  "v_cvt_pk_bf16_f32 v188, v232, v236\n" \
  "v_cvt_pk_bf16_f32 v189, v233, v237\n" \
  "v_cvt_pk_bf16_f32 v190, v234, v238\n" \
  "v_cvt_pk_bf16_f32 v191, v235, v239\n" \
  "v_and_b32 v188, s76, v188\n" \
  "v_and_b32 v189, s76, v189\n" \
  "v_and_b32 v190, s76, v190\n" \
  "v_and_b32 v191, s76, v191\n" \
  "buffer_store_dwordx4 v[188:191], v24, s[36:39], s63 offen sc1\n" \
  "v_pk_mul_f32 v[240:241], v[240:241], s[12:13] op_sel_hi:[1,0]\n" \
  "v_pk_mul_f32 v[242:243], v[242:243], s[12:13] op_sel_hi:[1,0]\n" \
  "v_pk_mul_f32 v[220:221], v[220:221], s[12:13] op_sel_hi:[1,0]\n" \
  "v_pk_mul_f32 v[222:223], v[222:223], s[12:13] op_sel_hi:[1,0]\n" \
  "v_cvt_pk_bf16_f32 v192, v240, v220\n" \
  "v_cvt_pk_bf16_f32 v193, v241, v221\n" \
  "v_cvt_pk_bf16_f32 v194, v242, v222\n" \
  "v_cvt_pk_bf16_f32 v195, v243, v223\n" \
  "v_and_b32 v192, s76, v192\n" \
  "v_and_b32 v193, s76, v193\n" \
  "v_and_b32 v194, s76, v194\n" \
  "v_and_b32 v195, s76, v195\n" \
  "buffer_store_dwordx4 v[192:195], v24, s[36:39], s63 offen offset:256 sc1\n" \
  "v_mov_b32 v6, v17\n" \
  "v_mov_b32 v5, v18\n" \
  "v_mov_b32 v7, v12\n" \
  "v_mov_b32 v4, v87\n" \
  "v_mov_b32 v25, v102\n" \
  "v_mov_b32 v160, v13\n" \
  "v_mov_b32 v161, v24\n" \
  "v_lshlrev_b32 v2, 13, v5\n" \
  "v_lshl_or_b32 v2, v6, 4, v2\n" \
  "s_lshl_b32 s60, s70, 0x2\n" \
  "s_add_u32 s60, s60, s3\n" \
  "s_lshl_b32 s61, s60, 0x8\n" \
  "v_add_u32 v14, s61, v2\n" \
  "v_add_u32 v15, 0x40000, v14\n" \
  "v_add_u32 v16, 0x80000, v14\n" \
  "v_add_u32 v17, 0xc0000, v14\n" \
  "s_lshl_b32 s51, s60, 0x5\n" \
  "s_waitcnt vmcnt(4)\n" \
  "v_cmp_eq_u64 vcc, s[48:49], v[26:27]\n" \
  "s_nop 0\n" \
  "s_cmp_eq_u64 vcc, -1\n" \
  "s_cbranch_scc0 LF0_slow%=\n" \
  "LF0_ok%=:\n" \
  "s_add_u32 s51, s51, s71\n" \
  "s_add_u32 m0, s11, 0x0\n" \
  "s_nop 0\n" \
  "buffer_load_dwordx4 v30, s[36:39], s4 offen sc1 lds\n" \
  "s_lshl_b32 s51, s51, 0x6\n" \
  "s_add_u32 m0, s11, 0x400\n" \
  "s_nop 0\n" \
  "buffer_load_dwordx4 v30, s[36:39], s5 offen sc1 lds\n" \
  "s_add_u32 s51, s51, 0x1a0d000\n" \
  "v_cmp_eq_u32 vcc, 0, v1\n" \
  "v_bfrev_b32 v12, 1\n" \
  "s_nop 0\n" \
  "v_cndmask_b32 v12, v12, 0, vcc\n" \
  "v_lshlrev_b32 v22, 4, v1\n" \
  "v_add_u32 v23, 0xe000, v22\n" \
  "s_lshl_b32 s60, s64, 0x2\n" \
  "s_add_u32 s60, s60, s72\n" \
  "s_lshl_b32 s61, s60, 0x8\n" \
  "v_add_u32 v18, s61, v2\n" \
  "s_mul_i32 s52, s60, 0x38000\n" \
  "s_mul_i32 s61, s3, 0x3800\n" \
  "s_add_u32 s52, s52, s61\n" \
  "s_add_u32 s52, s52, 0x1000000\n" \
  "s_lshl_b32 s60, s60, 0x5\n" \
  "s_lshl_b32 s61, s3, 0x3\n" \
  "s_add_u32 s60, s60, s61\n" \
  "s_add_u32 m0, s11, 0x800\n" \
  "s_nop 0\n" \
  "buffer_load_dwordx4 v30, s[36:39], s6 offen sc1 lds\n" \
  "s_lshl_b32 s60, s60, 0x6\n" \
  "s_add_u32 s60, s60, 0x1a0d000\n" \
  "v_and_b32 v3, 7, v1\n" \
  "v_lshl_add_u32 v13, v3, 6, s60\n" \
  "s_and_b32 s60, s3, 0x1\n" \
  "s_lshl_b32 s60, s60, 0x4\n" \
  "s_lshr_b32 s61, s3, 0x1\n" \
  "s_lshl_b32 s61, s61, 0x7\n" \
  "s_add_u32 s62, s60, s61\n" \
  "s_lshl_b32 s53, s62, 0xd\n" \
  "s_add_u32 s53, s53, 0xa00000\n" \
  "v_add_u32 v19, 0x40000, v18\n" \
  "v_add_u32 v20, 0x80000, v18\n" \
  "v_add_u32 v21, 0xc0000, v18\n" \
  "v_add_u32 v3, s62, v5\n" \
  "v_cvt_f32_u32 v162, v3\n" \
  "s_mul_i32 s60, s73, 0xe0\n" \
  "s_mul_i32 s61, s3, 0x38\n" \
  "s_add_u32 m0, s11, 0xc00\n" \
  "s_nop 0\n" \
  "buffer_load_dwordx4 v30, s[36:39], s7 offen sc1 lds\n" \
  "s_add_u32 s60, s60, s61\n" \
  "v_add_u32 v3, s60, v5\n" \
  "v_lshlrev_b32 v24, 10, v3\n" \
  "v_lshl_or_b32 v24, v6, 4, v24\n" \
  "s_lshl_b32 s60, s64, 0x1\n" \
  "s_lshr_b32 s61, s72, 0x1\n" \
  "s_add_u32 s60, s60, s61\n" \
  "s_lshl_b32 s60, s60, 0x2\n" \
  "s_lshr_b32 s61, s3, 0x1\n" \
  "s_add_u32 s60, s60, s61\n" \
  "s_lshl_b32 s60, s60, 0x5\n" \
  "s_and_b32 s61, s72, 0x1\n" \
  "s_lshl_b32 s61, s61, 0x4\n" \
  "s_add_u32 s60, s60, s61\n" \
  "s_lshl_b32 s60, s60, 0xa\n" \
  "s_lshl_b32 s61, s73, 0x7\n" \
  "s_add_u32 s60, s60, s61\n" \
  "s_and_b32 s61, s3, 0x1\n" \
  "s_add_u32 m0, s11, 0x1000\n" \
  "s_nop 0\n" \
  "buffer_load_dwordx4 v30, s[36:39], s8 offen sc1 lds\n" \
  "s_lshl_b32 s61, s61, 0x4\n" \
  "s_add_u32 s60, s60, s61\n" \
  "v_lshlrev_b32 v28, 10, v6\n" \
  "v_lshl_or_b32 v28, v5, 5, v28\n" \
  "v_add_u32 v28, s60, v28\n" \
  "v_add_u32 v29, 0x10000, v28\n" \
  "s_lshl_b32 s60, s3, 0xb\n" \
  "v_add_u32 v31, s60, v22\n" \
  "s_lshl_b32 s60, s3, 0xd\n" \
  "v_add_u32 v9, s60, v22\n" \
  "v_lshrrev_b32 v2, 2, v6\n" \
  "v_lshlrev_b32 v2, 4, v2\n" \
  "v_and_b32 v3, 3, v6\n" \
  "v_or_b32 v2, v2, v3\n" \
  "s_lshl_b32 s60, s73, 0x6\n" \
  "v_add_u32 v2, s60, v2\n" \
  "v_cvt_f32_u32 v163, v2\n" \
  "v_mul_f32 v163, 0x3b000000, v163\n" \
  "s_add_u32 m0, s11, 0x1400\n" \
  "s_nop 0\n" \
  "buffer_load_dwordx4 v30, s[36:39], s9 offen sc1 lds\n" \
  "v_and_b32 v2, 1, v1\n" \
  "v_cmp_ne_u32 s[54:55], 0, v2\n" \
  "v_and_b32 v2, 2, v1\n" \
  "v_cmp_ne_u32 s[56:57], 0, v2\n" \
  "v_lshrrev_b32 v2, 2, v6\n" \
  "v_cmp_eq_u32 s[58:59], v2, v5\n" \
  "v_and_b32 v2, 15, v1\n" \
  "v_lshrrev_b32 v3, 4, v1\n" \
  "v_lshl_or_b32 v2, v3, 5, v2\n" \
  "v_add_u32 v2, s62, v2\n" \
  "v_lshlrev_b32 v8, 6, v2\n" \
  "v_add_u32 v8, 0x1a09000, v8\n" \
  "s_lshl_b32 s60, s73, 0x2\n" \
  "s_add_u32 s60, s60, s3\n" \
  "s_lshl_b32 s60, s60, 0x9\n" \
  "s_add_u32 s60, s60, 0x1a09000\n" \
  "v_and_b32 v2, 7, v1\n" \
  "v_lshl_add_u32 v6, v2, 6, s60\n" \
  "s_add_u32 m0, s11, 0x1800\n" \
  "s_nop 0\n" \
  "buffer_load_dwordx4 v30, s[36:39], s10 offen sc1 lds\n" \
  "v_mov_b32 v60, v163\n" \
  "v_add_f32 v61, 0x3c000000, v163\n" \
  "v_add_f32 v62, 0x3c800000, v163\n" \
  "v_add_f32 v63, 0x3cc00000, v163\n" \
  "v_mul_f32 v64, v162, v60\n" \
  "v_mul_f32 v65, v162, v61\n" \
  "v_mul_f32 v66, v162, v62\n" \
  "v_mul_f32 v67, v162, v63\n" \
  "v_cos_f32 v32, v64\n" \
  "v_fmamk_f32 v35, v60, 0x42000000, v64\n" \
  "v_fmamk_f32 v37, v60, 0x42800000, v64\n" \
  "v_fmamk_f32 v39, v60, 0x42c00000, v64\n" \
  "v_cos_f32 v40, v65\n" \
  "v_fmamk_f32 v43, v61, 0x42000000, v65\n" \
  "v_fmamk_f32 v45, v61, 0x42800000, v65\n" \
  "v_fmamk_f32 v47, v61, 0x42c00000, v65\n" \
  "s_add_u32 m0, s11, 0x1800\n" \
  "s_nop 0\n" \
  "buffer_load_dwordx4 v30, s[36:39], s4 offen offset:1024 sc1 lds\n" \
  "v_sin_f32 v33, -v64\n" \
  "v_cos_f32 v34, v35\n" \
  "v_cos_f32 v36, v37\n" \
  "v_cos_f32 v38, v39\n" \
  "v_sin_f32 v41, -v65\n" \
  "v_cos_f32 v42, v43\n" \
  "v_cos_f32 v44, v45\n" \
  "v_cos_f32 v46, v47\n" \
  "v_cvt_pk_bf16_f32 v196, v32, v33\n" \
  "v_sin_f32 v35, -v35\n" \
  "s_add_u32 m0, s11, 0x1c00\n" \
  "s_nop 0\n" \
  "buffer_load_dwordx4 v30, s[36:39], s5 offen offset:1024 sc1 lds\n" \
  "v_sin_f32 v37, -v37\n" \
  "v_sin_f32 v39, -v39\n" \
  "v_cvt_pk_bf16_f32 v200, v40, v41\n" \
  "v_sin_f32 v43, -v43\n" \
  "v_sin_f32 v45, -v45\n" \
  "v_sin_f32 v47, -v47\n" \
  "v_cvt_pk_bf16_f32 v197, v34, v35\n" \
  "v_cvt_pk_bf16_f32 v198, v36, v37\n" \
  "v_cvt_pk_bf16_f32 v199, v38, v39\n" \
  "v_cvt_pk_bf16_f32 v201, v42, v43\n" \
  "v_cvt_pk_bf16_f32 v202, v44, v45\n" \
  "v_cvt_pk_bf16_f32 v203, v46, v47\n" \
  "s_add_u32 m0, s11, 0x2000\n" \
  "s_nop 0\n" \
  "buffer_load_dwordx4 v30, s[36:39], s6 offen offset:1024 sc1 lds\n" \
  "v_cos_f32 v32, v66\n" \
  "v_fmamk_f32 v35, v62, 0x42000000, v66\n" \
  "v_fmamk_f32 v37, v62, 0x42800000, v66\n" \
  "v_fmamk_f32 v39, v62, 0x42c00000, v66\n" \
  "v_cos_f32 v40, v67\n" \
  "v_fmamk_f32 v43, v63, 0x42000000, v67\n" \
  "v_fmamk_f32 v45, v63, 0x42800000, v67\n" \
  "v_fmamk_f32 v47, v63, 0x42c00000, v67\n" \
  "v_sin_f32 v33, -v66\n" \
  "v_cos_f32 v34, v35\n" \
  "v_cos_f32 v36, v37\n" \
  "v_cos_f32 v38, v39\n" \
  "s_add_u32 m0, s11, 0x2400\n" \
  "s_nop 0\n" \
  "buffer_load_dwordx4 v30, s[36:39], s7 offen offset:1024 sc1 lds\n" \
  "v_sin_f32 v41, -v67\n" \
  "v_cos_f32 v42, v43\n" \
  "v_cos_f32 v44, v45\n" \
  "v_cos_f32 v46, v47\n" \
  "v_cvt_pk_bf16_f32 v204, v32, v33\n" \
  "v_sin_f32 v35, -v35\n" \
  "v_sin_f32 v37, -v37\n" \
  "v_sin_f32 v39, -v39\n" \
  "v_cvt_pk_bf16_f32 v208, v40, v41\n" \
  "v_sin_f32 v43, -v43\n" \
  "s_add_u32 m0, s11, 0x2800\n" \
  "s_nop 0\n" \
  "buffer_load_dwordx4 v30, s[36:39], s8 offen offset:1024 sc1 lds\n" \
  "v_sin_f32 v45, -v45\n" \
  "v_sin_f32 v47, -v47\n" \
  "v_cvt_pk_bf16_f32 v205, v34, v35\n" \
  "v_cvt_pk_bf16_f32 v206, v36, v37\n" \
  "v_cvt_pk_bf16_f32 v207, v38, v39\n" \
  "v_cvt_pk_bf16_f32 v209, v42, v43\n" \
  "v_cvt_pk_bf16_f32 v210, v44, v45\n" \
  "v_cvt_pk_bf16_f32 v211, v46, v47\n" \
  "v_mov_b32 v2, 0x3f35\n" \
  "v_mov_b32 v3, 0xbf35\n" \
  "v_cndmask_b32 v2, v2, v3, s[54:55]\n" \
  "v_cmp_gt_u32 vcc, 16, v0\n" \
  "s_nop 1\n" \
  "v_cndmask_b32 v196, v196, v2, vcc\n" \
  "v_cndmask_b32 v200, v200, v2, vcc\n" \
  "v_cndmask_b32 v204, v204, v2, vcc\n" \
  "v_cndmask_b32 v208, v208, v2, vcc\n" \
  "s_add_u32 m0, s11, 0x2c00\n" \
  "s_nop 0\n" \
  "buffer_load_dwordx4 v30, s[36:39], s9 offen offset:1024 sc1 lds\n" \
  "v_fmamk_f32 v33, v60, 0x40800000, v64\n" \
  "v_fmamk_f32 v35, v60, 0x42100000, v64\n" \
  "v_fmamk_f32 v37, v60, 0x42880000, v64\n" \
  "v_fmamk_f32 v39, v60, 0x42c80000, v64\n" \
  "v_fmamk_f32 v41, v61, 0x40800000, v65\n" \
  "v_fmamk_f32 v43, v61, 0x42100000, v65\n" \
  "v_fmamk_f32 v45, v61, 0x42880000, v65\n" \
  "v_fmamk_f32 v47, v61, 0x42c80000, v65\n" \
  "v_cos_f32 v32, v33\n" \
  "v_cos_f32 v34, v35\n" \
  "v_cos_f32 v36, v37\n" \
  "v_cos_f32 v38, v39\n" \
  "v_cos_f32 v40, v41\n" \
  "s_add_u32 m0, s11, 0x3000\n" \
  "s_nop 0\n" \
  "buffer_load_dwordx4 v30, s[36:39], s10 offen offset:1024 sc1 lds\n" \
  "v_cos_f32 v42, v43\n" \
  "v_cos_f32 v44, v45\n" \
  "v_cos_f32 v46, v47\n" \
  "v_sin_f32 v33, -v33\n" \
  "v_sin_f32 v35, -v35\n" \
  "v_sin_f32 v37, -v37\n" \
  "v_sin_f32 v39, -v39\n" \
  "v_sin_f32 v41, -v41\n" \
  "v_sin_f32 v43, -v43\n" \
  "s_add_u32 m0, s11, 0xe000\n" \
  "s_add_u32 s13, s4, 0x2000\n" \
  "buffer_load_dwordx4 v30, s[36:39], s13 offen sc1 lds\n" \
  "v_sin_f32 v45, -v45\n" \
  "v_sin_f32 v47, -v47\n" \
  "v_cvt_pk_bf16_f32 v212, v32, v33\n" \
  "v_cvt_pk_bf16_f32 v213, v34, v35\n" \
  "v_cvt_pk_bf16_f32 v214, v36, v37\n" \
  "v_cvt_pk_bf16_f32 v215, v38, v39\n" \
  "v_cvt_pk_bf16_f32 v216, v40, v41\n" \
  "v_cvt_pk_bf16_f32 v217, v42, v43\n" \
  "v_cvt_pk_bf16_f32 v218, v44, v45\n" \
  "v_cvt_pk_bf16_f32 v219, v46, v47\n" \
  "v_fmamk_f32 v33, v62, 0x40800000, v66\n" \
  "v_fmamk_f32 v35, v62, 0x42100000, v66\n" \
  "v_fmamk_f32 v37, v62, 0x42880000, v66\n" \
  "v_fmamk_f32 v39, v62, 0x42c80000, v66\n" \
  "v_fmamk_f32 v41, v63, 0x40800000, v67\n" \
  "s_add_u32 m0, s11, 0xe400\n" \
  "s_add_u32 s13, s5, 0x2000\n" \
  "buffer_load_dwordx4 v30, s[36:39], s13 offen sc1 lds\n" \
  "v_fmamk_f32 v43, v63, 0x42100000, v67\n" \
  "v_fmamk_f32 v45, v63, 0x42880000, v67\n" \
  "v_fmamk_f32 v47, v63, 0x42c80000, v67\n" \
  "v_cos_f32 v32, v33\n" \
  "v_cos_f32 v34, v35\n" \
  "v_cos_f32 v36, v37\n" \
  "v_cos_f32 v38, v39\n" \
  "v_cos_f32 v40, v41\n" \
  "v_cos_f32 v42, v43\n" \
  "v_cos_f32 v44, v45\n" \
  "s_add_u32 m0, s11, 0xe800\n" \
  "s_add_u32 s13, s6, 0x2000\n" \
  "buffer_load_dwordx4 v30, s[36:39], s13 offen sc1 lds\n" \
  "v_cos_f32 v46, v47\n" \
  "v_sin_f32 v33, -v33\n" \
  "v_sin_f32 v35, -v35\n" \
  "v_sin_f32 v37, -v37\n" \
  "v_sin_f32 v39, -v39\n" \
  "v_sin_f32 v41, -v41\n" \
  "v_sin_f32 v43, -v43\n" \
  "v_sin_f32 v45, -v45\n" \
  "v_sin_f32 v47, -v47\n" \
  "s_mov_b32 s14, 0x800000\n" \
  "buffer_load_dwordx4 a[56:59], v14, s[36:39], s14 offen sc1\n" \
  "v_cvt_pk_bf16_f32 v220, v32, v33\n" \
  "v_cvt_pk_bf16_f32 v221, v34, v35\n" \
  "v_cvt_pk_bf16_f32 v222, v36, v37\n" \
  "v_cvt_pk_bf16_f32 v223, v38, v39\n" \
  "v_cvt_pk_bf16_f32 v224, v40, v41\n" \
  "v_cvt_pk_bf16_f32 v225, v42, v43\n" \
  "v_cvt_pk_bf16_f32 v226, v44, v45\n" \
  "v_cvt_pk_bf16_f32 v227, v46, v47\n" \
  "buffer_load_dwordx4 a[60:63], v15, s[36:39], s14 offen sc1\n" \
  "buffer_load_dwordx4 a[64:67], v16, s[36:39], s14 offen sc1\n" \
  "buffer_load_dwordx4 a[68:71], v17, s[36:39], s14 offen sc1\n" \
  "s_mov_b32 s14, 0x808000\n" \
  "buffer_load_dwordx4 a[72:75], v14, s[36:39], s14 offen sc1\n" \
  "buffer_load_dwordx4 a[76:79], v15, s[36:39], s14 offen sc1\n" \
  "buffer_load_dwordx4 a[80:83], v16, s[36:39], s14 offen sc1\n" \
  "buffer_load_dwordx4 a[84:87], v17, s[36:39], s14 offen sc1\n" \
  "s_waitcnt vmcnt(11)\n" \
  "s_barrier\n" \
  "s_lshl_b32 s60, s66, 0x6\n" \
  "s_add_u32 s60, s60, 0x1a09000\n" \
  "buffer_store_dwordx2 v[10:11], v7, s[36:39], s60 offen sc1\n" \
  "ds_read_b128 a[0:3], v22\n" \
  "ds_read_b128 a[4:7], v22 offset:1024\n" \
  "ds_read_b128 a[8:11], v22 offset:2048\n" \
  "ds_read_b128 a[12:15], v22 offset:3072\n" \
  "ds_read_b128 a[16:19], v22 offset:4096\n" \
  "ds_read_b128 a[20:23], v22 offset:5120\n" \
  "ds_read_b128 a[24:27], v22 offset:6144\n" \
  "s_waitcnt vmcnt(5)\n" \
  "v_accvgpr_read_b32 v164, a56\n" \
  "v_accvgpr_read_b32 v168, a57\n" \
  "v_accvgpr_read_b32 v172, a58\n" \
  "v_accvgpr_read_b32 v176, a59\n" \
  "v_accvgpr_read_b32 v165, a60\n" \
  "v_accvgpr_read_b32 v169, a61\n" \
  "v_accvgpr_read_b32 v173, a62\n" \
  "v_accvgpr_read_b32 v177, a63\n" \
  "v_accvgpr_read_b32 v166, a64\n" \
  "v_accvgpr_read_b32 v170, a65\n" \
  "v_accvgpr_read_b32 v174, a66\n" \
  "v_accvgpr_read_b32 v178, a67\n" \
  "v_accvgpr_read_b32 v167, a68\n" \
  "v_accvgpr_read_b32 v171, a69\n" \
  "v_accvgpr_read_b32 v175, a70\n" \
  "v_accvgpr_read_b32 v179, a71\n" \
  "v_max3_u32 v2, v164, v165, v166\n" \
  "v_max_u32 v2, v2, v167\n" \
  "v_cmp_ne_u32 vcc, -1, v2\n" \
  "s_nop 0\n" \
  "s_cmp_eq_u64 vcc, -1\n" \
  "s_cbranch_scc0 LS0_slow%=\n" \
  "LS0_ok%=:\n" \
  "s_waitcnt lgkmcnt(4)\n" \
  "v_mfma_f32_16x16x32_bf16 v[32:35], a[0:3], v[164:167], 0\n" \
  "s_mov_b32 s14, 0x810000\n" \
  "buffer_load_dwordx4 a[88:91], v14, s[36:39], s14 offen sc1\n" \
  "v_mfma_f32_16x16x32_bf16 v[36:39], a[0:3], v[168:171], 0\n" \
  "buffer_load_dwordx4 a[92:95], v15, s[36:39], s14 offen sc1\n" \
  "ds_read_b128 a[28:31], v22 offset:7168\n" \
  "v_mfma_f32_16x16x32_bf16 v[88:91], a[0:3], v[172:175], 0\n" \
  "ds_read_b128 a[32:35], v22 offset:8192\n" \
  "ds_read_b128 a[36:39], v22 offset:9216\n" \
  "v_mfma_f32_16x16x32_bf16 v[92:95], a[0:3], v[176:179], 0\n" \
  "buffer_load_dwordx4 a[96:99], v16, s[36:39], s14 offen sc1\n" \
  "ds_read_b128 a[40:43], v22 offset:10240\n" \
  "v_mfma_f32_16x16x32_bf16 v[100:103], a[4:7], v[176:179], 0\n" \
  "ds_read_b128 a[44:47], v22 offset:11264\n" \
  "ds_read_b128 a[48:51], v22 offset:12288\n" \
  "v_mfma_f32_16x16x32_bf16 v[96:99], a[4:7], v[172:175], 0\n" \
  "ds_read_b128 a[52:55], v22 offset:13312\n" \
  "s_waitcnt vmcnt(4)\n" \
  "v_mfma_f32_16x16x32_bf16 v[44:47], a[4:7], v[168:171], 0\n" \
  "v_accvgpr_read_b32 v180, a72\n" \
  "v_accvgpr_read_b32 v184, a73\n" \
  "v_mfma_f32_16x16x32_bf16 v[40:43], a[4:7], v[164:167], 0\n" \
  "buffer_load_dwordx4 a[100:103], v17, s[36:39], s14 offen sc1\n" \
  "v_accvgpr_read_b32 v188, a74\n" \
  "v_mfma_f32_16x16x32_bf16 v[48:51], a[8:11], v[164:167], 0\n" \
  "v_accvgpr_read_b32 v192, a75\n" \
  "v_accvgpr_read_b32 v181, a76\n" \
  "v_mfma_f32_16x16x32_bf16 v[52:55], a[8:11], v[168:171], 0\n" \
  "v_accvgpr_read_b32 v185, a77\n" \
  "v_accvgpr_read_b32 v189, a78\n" \
  "v_mfma_f32_16x16x32_bf16 v[104:107], a[8:11], v[172:175], 0\n" \
  "v_accvgpr_read_b32 v193, a79\n" \
  "v_accvgpr_read_b32 v182, a80\n" \
  "v_mfma_f32_16x16x32_bf16 v[108:111], a[8:11], v[176:179], 0\n" \
  "s_mov_b32 s14, 0x818000\n" \
  "buffer_load_dwordx4 a[104:107], v14, s[36:39], s14 offen sc1\n" \
  "s_waitcnt lgkmcnt(7)\n" \
  "v_mfma_f32_16x16x32_bf16 v[116:119], a[12:15], v[176:179], 0\n" \
  "v_accvgpr_read_b32 v186, a81\n" \
  "v_accvgpr_read_b32 v190, a82\n" \
  "v_mfma_f32_16x16x32_bf16 v[112:115], a[12:15], v[172:175], 0\n" \
  "v_accvgpr_read_b32 v194, a83\n" \
  "v_accvgpr_read_b32 v183, a84\n" \
  "v_mfma_f32_16x16x32_bf16 v[60:63], a[12:15], v[168:171], 0\n" \
  "v_accvgpr_read_b32 v187, a85\n" \
  "v_accvgpr_read_b32 v191, a86\n" \
  "v_mfma_f32_16x16x32_bf16 v[56:59], a[12:15], v[164:167], 0\n" \
  "buffer_load_dwordx4 a[108:111], v15, s[36:39], s14 offen sc1\n" \
  "v_accvgpr_read_b32 v195, a87\n" \
  "v_mfma_f32_16x16x32_bf16 v[64:67], a[16:19], v[164:167], 0\n" \
  "v_max3_u32 v2, v180, v181, v182\n" \
  "v_max_u32 v2, v2, v183\n" \
  "v_mfma_f32_16x16x32_bf16 v[68:71], a[16:19], v[168:171], 0\n" \
  "v_cmp_ne_u32 vcc, -1, v2\n" \
  "s_nop 0\n" \
  "s_cmp_eq_u64 vcc, -1\n" \
  "v_mfma_f32_16x16x32_bf16 v[120:123], a[16:19], v[172:175], 0\n" \
  "s_cbranch_scc0 LS1_slow%=\n" \
  "LS1_ok%=:\n" \
  "v_mfma_f32_16x16x32_bf16 v[124:127], a[16:19], v[176:179], 0\n" \
  "buffer_load_dwordx4 a[112:115], v16, s[36:39], s14 offen sc1\n" \
  "v_mfma_f32_16x16x32_bf16 v[132:135], a[20:23], v[176:179], 0\n" \
  "v_mfma_f32_16x16x32_bf16 v[128:131], a[20:23], v[172:175], 0\n" \
  "v_mfma_f32_16x16x32_bf16 v[76:79], a[20:23], v[168:171], 0\n" \
  "v_mfma_f32_16x16x32_bf16 v[72:75], a[20:23], v[164:167], 0\n" \
  "buffer_load_dwordx4 a[116:119], v17, s[36:39], s14 offen sc1\n" \
  "v_mfma_f32_16x16x32_bf16 v[80:83], a[24:27], v[164:167], 0\n" \
  "v_mfma_f32_16x16x32_bf16 v[84:87], a[24:27], v[168:171], 0\n" \
  "v_mfma_f32_16x16x32_bf16 v[136:139], a[24:27], v[172:175], 0\n" \
  "v_mfma_f32_16x16x32_bf16 v[140:143], a[24:27], v[176:179], 0\n" \
  "s_mov_b32 s14, 0x820000\n" \
  "buffer_load_dwordx4 a[120:123], v14, s[36:39], s14 offen sc1\n" \
  "v_mfma_f32_16x16x32_bf16 v[156:159], v[176:179], v[176:179], 0\n" \
  "v_mfma_f32_16x16x32_bf16 v[152:155], v[172:175], v[172:175], 0\n" \
  "v_mfma_f32_16x16x32_bf16 v[148:151], v[168:171], v[168:171], 0\n" \
  "v_mfma_f32_16x16x32_bf16 v[144:147], v[164:167], v[164:167], 0\n" \
  "buffer_load_dwordx4 a[124:127], v15, s[36:39], s14 offen sc1\n" \
  "s_waitcnt lgkmcnt(4)\n" \
  "v_mfma_f32_16x16x32_bf16 v[32:35], a[28:31], v[180:183], v[32:35]\n" \
  "ds_read_b128 a[0:3], v22 offset:14336\n" \
  "ds_read_b128 a[4:7], v22 offset:15360\n" \
  "v_mfma_f32_16x16x32_bf16 v[36:39], a[28:31], v[184:187], v[36:39]\n" \
  "ds_read_b128 a[8:11], v22 offset:16384\n" \
  "ds_read_b128 a[12:15], v22 offset:17408\n" \
  "v_mfma_f32_16x16x32_bf16 v[88:91], a[28:31], v[188:191], v[88:91]\n" \
  "buffer_load_dwordx4 a[128:131], v16, s[36:39], s14 offen sc1\n" \
  "ds_read_b128 a[16:19], v22 offset:18432\n" \
  "v_mfma_f32_16x16x32_bf16 v[92:95], a[28:31], v[192:195], v[92:95]\n" \
  "ds_read_b128 a[20:23], v22 offset:19456\n" \
  "ds_read_b128 a[24:27], v22 offset:20480\n" \
  "v_mfma_f32_16x16x32_bf16 v[100:103], a[32:35], v[192:195], v[100:103]\n" \
  "s_waitcnt vmcnt(7)\n" \
  "v_accvgpr_read_b32 v164, a88\n" \
  "v_mfma_f32_16x16x32_bf16 v[96:99], a[32:35], v[188:191], v[96:99]\n" \
  "v_accvgpr_read_b32 v168, a89\n" \
  "v_accvgpr_read_b32 v172, a90\n" \
  "v_mfma_f32_16x16x32_bf16 v[44:47], a[32:35], v[184:187], v[44:47]\n" \
  "buffer_load_dwordx4 a[132:135], v17, s[36:39], s14 offen sc1\n" \
  "v_accvgpr_read_b32 v176, a91\n" \
  "v_mfma_f32_16x16x32_bf16 v[40:43], a[32:35], v[180:183], v[40:43]\n" \
  "v_accvgpr_read_b32 v165, a92\n" \
  "v_accvgpr_read_b32 v169, a93\n" \
  "v_mfma_f32_16x16x32_bf16 v[48:51], a[36:39], v[180:183], v[48:51]\n" \
  "v_accvgpr_read_b32 v173, a94\n" \
  "v_accvgpr_read_b32 v177, a95\n" \
  "v_mfma_f32_16x16x32_bf16 v[52:55], a[36:39], v[184:187], v[52:55]\n" \
  "v_accvgpr_read_b32 v166, a96\n" \
  "v_accvgpr_read_b32 v170, a97\n" \
  "v_mfma_f32_16x16x32_bf16 v[104:107], a[36:39], v[188:191], v[104:107]\n" \
  "s_add_u32 m0, s11, 0xec00\n" \
  "s_add_u32 s13, s7, 0x2000\n" \
  "buffer_load_dwordx4 v30, s[36:39], s13 offen sc1 lds\n" \
  "v_mfma_f32_16x16x32_bf16 v[108:111], a[36:39], v[192:195], v[108:111]\n" \
  "v_accvgpr_read_b32 v174, a98\n" \
  "v_accvgpr_read_b32 v178, a99\n" \
  "s_waitcnt lgkmcnt(7)\n" \
  "v_mfma_f32_16x16x32_bf16 v[116:119], a[40:43], v[192:195], v[116:119]\n" \
  "v_accvgpr_read_b32 v167, a100\n" \
  "v_accvgpr_read_b32 v171, a101\n" \
  "v_mfma_f32_16x16x32_bf16 v[112:115], a[40:43], v[188:191], v[112:115]\n" \
  "v_accvgpr_read_b32 v175, a102\n" \
  "v_accvgpr_read_b32 v179, a103\n" \
  "v_mfma_f32_16x16x32_bf16 v[60:63], a[40:43], v[184:187], v[60:63]\n" \
  "v_max3_u32 v2, v164, v165, v166\n" \
  "v_max_u32 v2, v2, v167\n" \
  "v_mfma_f32_16x16x32_bf16 v[56:59], a[40:43], v[180:183], v[56:59]\n" \
  "s_add_u32 m0, s11, 0xf000\n" \
  "s_add_u32 s13, s8, 0x2000\n" \
  "buffer_load_dwordx4 v30, s[36:39], s13 offen sc1 lds\n" \
  "v_mfma_f32_16x16x32_bf16 v[64:67], a[44:47], v[180:183], v[64:67]\n" \
  "v_cmp_ne_u32 vcc, -1, v2\n" \
  "s_nop 0\n" \
  "s_cmp_eq_u64 vcc, -1\n" \
  "v_mfma_f32_16x16x32_bf16 v[68:71], a[44:47], v[184:187], v[68:71]\n" \
  "s_cbranch_scc0 LS2_slow%=\n" \
  "LS2_ok%=:\n" \
  "v_mfma_f32_16x16x32_bf16 v[120:123], a[44:47], v[188:191], v[120:123]\n" \
  "v_mfma_f32_16x16x32_bf16 v[124:127], a[44:47], v[192:195], v[124:127]\n" \
  "v_mfma_f32_16x16x32_bf16 v[132:135], a[48:51], v[192:195], v[132:135]\n" \
  "s_add_u32 m0, s11, 0xf400\n" \
  "s_add_u32 s13, s9, 0x2000\n" \
  "buffer_load_dwordx4 v30, s[36:39], s13 offen sc1 lds\n" \
  "v_mfma_f32_16x16x32_bf16 v[128:131], a[48:51], v[188:191], v[128:131]\n" \
  "v_mfma_f32_16x16x32_bf16 v[76:79], a[48:51], v[184:187], v[76:79]\n" \
  "v_mfma_f32_16x16x32_bf16 v[72:75], a[48:51], v[180:183], v[72:75]\n" \
  "v_mfma_f32_16x16x32_bf16 v[80:83], a[52:55], v[180:183], v[80:83]\n" \
  "s_add_u32 m0, s11, 0xf800\n" \
  "s_add_u32 s13, s10, 0x2000\n" \
  "buffer_load_dwordx4 v30, s[36:39], s13 offen sc1 lds\n" \
  "v_mfma_f32_16x16x32_bf16 v[84:87], a[52:55], v[184:187], v[84:87]\n" \
  "v_mfma_f32_16x16x32_bf16 v[136:139], a[52:55], v[188:191], v[136:139]\n" \
  "v_mfma_f32_16x16x32_bf16 v[140:143], a[52:55], v[192:195], v[140:143]\n" \
  "v_mfma_f32_16x16x32_bf16 v[156:159], v[192:195], v[192:195], v[156:159]\n" \
  "v_mfma_f32_16x16x32_bf16 v[152:155], v[188:191], v[188:191], v[152:155]\n" \
  "s_add_u32 m0, s11, 0xf800\n" \
  "s_add_u32 s13, s4, 0x2000\n" \
  "buffer_load_dwordx4 v30, s[36:39], s13 offen offset:1024 sc1 lds\n" \
  "v_mfma_f32_16x16x32_bf16 v[148:151], v[184:187], v[184:187], v[148:151]\n" \
  "v_mfma_f32_16x16x32_bf16 v[144:147], v[180:183], v[180:183], v[144:147]\n" \
  "s_waitcnt lgkmcnt(4)\n" \
  "v_mfma_f32_16x16x32_bf16 v[32:35], a[0:3], v[164:167], v[32:35]\n" \
  "ds_read_b128 a[28:31], v22 offset:21504\n" \
  "ds_read_b128 a[32:35], v22 offset:22528\n" \
  "v_mfma_f32_16x16x32_bf16 v[36:39], a[0:3], v[168:171], v[36:39]\n" \
  "ds_read_b128 a[36:39], v22 offset:23552\n" \
  "ds_read_b128 a[40:43], v22 offset:24576\n" \
  "v_mfma_f32_16x16x32_bf16 v[88:91], a[0:3], v[172:175], v[88:91]\n" \
  "s_add_u32 m0, s11, 0xfc00\n" \
  "s_add_u32 s13, s5, 0x2000\n" \
  "buffer_load_dwordx4 v30, s[36:39], s13 offen offset:1024 sc1 lds\n" \
  "v_mfma_f32_16x16x32_bf16 v[92:95], a[0:3], v[176:179], v[92:95]\n" \
  "ds_read_b128 a[44:47], v22 offset:25600\n" \
  "ds_read_b128 a[48:51], v22 offset:26624\n" \
  "v_mfma_f32_16x16x32_bf16 v[100:103], a[4:7], v[176:179], v[100:103]\n" \
  "ds_read_b128 a[52:55], v22 offset:27648\n" \
  "s_waitcnt vmcnt(10)\n" \
  "v_mfma_f32_16x16x32_bf16 v[96:99], a[4:7], v[172:175], v[96:99]\n" \
  "v_accvgpr_read_b32 v180, a104\n" \
  "v_accvgpr_read_b32 v184, a105\n" \
  "v_mfma_f32_16x16x32_bf16 v[44:47], a[4:7], v[168:171], v[44:47]\n" \
  "s_add_u32 m0, s11, 0x10000\n" \
  "s_add_u32 s13, s6, 0x2000\n" \
  "buffer_load_dwordx4 v30, s[36:39], s13 offen offset:1024 sc1 lds\n" \
  "v_mfma_f32_16x16x32_bf16 v[40:43], a[4:7], v[164:167], v[40:43]\n" \
  "v_accvgpr_read_b32 v188, a106\n" \
  "v_accvgpr_read_b32 v192, a107\n" \
  "v_mfma_f32_16x16x32_bf16 v[48:51], a[8:11], v[164:167], v[48:51]\n" \
  "v_accvgpr_read_b32 v181, a108\n" \
  "v_accvgpr_read_b32 v185, a109\n" \
  "v_mfma_f32_16x16x32_bf16 v[52:55], a[8:11], v[168:171], v[52:55]\n" \
  "v_accvgpr_read_b32 v189, a110\n" \
  "v_accvgpr_read_b32 v193, a111\n" \
  "v_mfma_f32_16x16x32_bf16 v[104:107], a[8:11], v[172:175], v[104:107]\n" \
  "v_accvgpr_read_b32 v182, a112\n" \
  "v_accvgpr_read_b32 v186, a113\n" \
  "v_mfma_f32_16x16x32_bf16 v[108:111], a[8:11], v[176:179], v[108:111]\n" \
  "s_add_u32 m0, s11, 0x10400\n" \
  "s_add_u32 s13, s7, 0x2000\n" \
  "buffer_load_dwordx4 v30, s[36:39], s13 offen offset:1024 sc1 lds\n" \
  "s_waitcnt lgkmcnt(7)\n" \
  "v_mfma_f32_16x16x32_bf16 v[116:119], a[12:15], v[176:179], v[116:119]\n" \
  "v_accvgpr_read_b32 v190, a114\n" \
  "v_accvgpr_read_b32 v194, a115\n" \
  "v_mfma_f32_16x16x32_bf16 v[112:115], a[12:15], v[172:175], v[112:115]\n" \
  "v_accvgpr_read_b32 v183, a116\n" \
  "v_accvgpr_read_b32 v187, a117\n" \
  "v_mfma_f32_16x16x32_bf16 v[60:63], a[12:15], v[168:171], v[60:63]\n" \
  "v_accvgpr_read_b32 v191, a118\n" \
  "v_accvgpr_read_b32 v195, a119\n" \
  "v_mfma_f32_16x16x32_bf16 v[56:59], a[12:15], v[164:167], v[56:59]\n" \
  "v_max3_u32 v2, v180, v181, v182\n" \
  "v_max_u32 v2, v2, v183\n" \
  "v_mfma_f32_16x16x32_bf16 v[64:67], a[16:19], v[164:167], v[64:67]\n" \
  "s_add_u32 m0, s11, 0x10800\n" \
  "s_add_u32 s13, s8, 0x2000\n" \
  "buffer_load_dwordx4 v30, s[36:39], s13 offen offset:1024 sc1 lds\n" \
  "v_mfma_f32_16x16x32_bf16 v[68:71], a[16:19], v[168:171], v[68:71]\n" \
  "v_cmp_ne_u32 vcc, -1, v2\n" \
  "s_nop 0\n" \
  "s_cmp_eq_u64 vcc, -1\n" \
  "v_mfma_f32_16x16x32_bf16 v[120:123], a[16:19], v[172:175], v[120:123]\n" \
  "s_cbranch_scc0 LS3_slow%=\n" \
  "LS3_ok%=:\n" \
  "v_mfma_f32_16x16x32_bf16 v[124:127], a[16:19], v[176:179], v[124:127]\n" \
  "v_mfma_f32_16x16x32_bf16 v[132:135], a[20:23], v[176:179], v[132:135]\n" \
  "s_add_u32 m0, s11, 0x10c00\n" \
  "s_add_u32 s13, s9, 0x2000\n" \
  "buffer_load_dwordx4 v30, s[36:39], s13 offen offset:1024 sc1 lds\n" \
  "v_mfma_f32_16x16x32_bf16 v[128:131], a[20:23], v[172:175], v[128:131]\n" \
  "v_mfma_f32_16x16x32_bf16 v[76:79], a[20:23], v[168:171], v[76:79]\n" \
  "v_mfma_f32_16x16x32_bf16 v[72:75], a[20:23], v[164:167], v[72:75]\n" \
  "v_mfma_f32_16x16x32_bf16 v[80:83], a[24:27], v[164:167], v[80:83]\n" \
  "v_mfma_f32_16x16x32_bf16 v[84:87], a[24:27], v[168:171], v[84:87]\n" \
  "s_add_u32 m0, s11, 0x11000\n" \
  "s_add_u32 s13, s10, 0x2000\n" \
  "buffer_load_dwordx4 v30, s[36:39], s13 offen offset:1024 sc1 lds\n" \
  "v_mfma_f32_16x16x32_bf16 v[136:139], a[24:27], v[172:175], v[136:139]\n" \
  "v_mfma_f32_16x16x32_bf16 v[140:143], a[24:27], v[176:179], v[140:143]\n" \
  "v_mfma_f32_16x16x32_bf16 v[156:159], v[176:179], v[176:179], v[156:159]\n" \
  "v_mfma_f32_16x16x32_bf16 v[152:155], v[172:175], v[172:175], v[152:155]\n" \
  "v_mfma_f32_16x16x32_bf16 v[148:151], v[168:171], v[168:171], v[148:151]\n" \
  "s_mov_b32 s14, 0x828000\n" \
  "buffer_load_dwordx4 a[56:59], v14, s[36:39], s14 offen sc1\n" \
  "v_mfma_f32_16x16x32_bf16 v[144:147], v[164:167], v[164:167], v[144:147]\n" \
  "s_waitcnt lgkmcnt(4)\n" \
  "v_mfma_f32_16x16x32_bf16 v[32:35], a[28:31], v[180:183], v[32:35]\n" \
  "ds_read_b128 a[0:3], v22 offset:28672\n" \
  "ds_read_b128 a[4:7], v22 offset:29696\n" \
  "v_mfma_f32_16x16x32_bf16 v[36:39], a[28:31], v[184:187], v[36:39]\n" \
  "ds_read_b128 a[8:11], v22 offset:30720\n" \
  "ds_read_b128 a[12:15], v22 offset:31744\n" \
  "v_mfma_f32_16x16x32_bf16 v[88:91], a[28:31], v[188:191], v[88:91]\n" \
  "buffer_load_dwordx4 a[60:63], v15, s[36:39], s14 offen sc1\n" \
  "ds_read_b128 a[16:19], v22 offset:32768\n" \
  "v_mfma_f32_16x16x32_bf16 v[92:95], a[28:31], v[192:195], v[92:95]\n" \
  "ds_read_b128 a[20:23], v22 offset:33792\n" \
  "ds_read_b128 a[24:27], v22 offset:34816\n" \
  "v_mfma_f32_16x16x32_bf16 v[100:103], a[32:35], v[192:195], v[100:103]\n" \
  "s_waitcnt vmcnt(13)\n" \
  "v_accvgpr_read_b32 v164, a120\n" \
  "v_mfma_f32_16x16x32_bf16 v[96:99], a[32:35], v[188:191], v[96:99]\n" \
  "v_accvgpr_read_b32 v168, a121\n" \
  "v_accvgpr_read_b32 v172, a122\n" \
  "v_mfma_f32_16x16x32_bf16 v[44:47], a[32:35], v[184:187], v[44:47]\n" \
  "buffer_load_dwordx4 a[64:67], v16, s[36:39], s14 offen sc1\n" \
  "v_accvgpr_read_b32 v176, a123\n" \
  "v_mfma_f32_16x16x32_bf16 v[40:43], a[32:35], v[180:183], v[40:43]\n" \
  "v_accvgpr_read_b32 v165, a124\n" \
  "v_accvgpr_read_b32 v169, a125\n" \
  "v_mfma_f32_16x16x32_bf16 v[48:51], a[36:39], v[180:183], v[48:51]\n" \
  "v_accvgpr_read_b32 v173, a126\n" \
  "v_accvgpr_read_b32 v177, a127\n" \
  "v_mfma_f32_16x16x32_bf16 v[52:55], a[36:39], v[184:187], v[52:55]\n" \
  "v_accvgpr_read_b32 v166, a128\n" \
  "v_accvgpr_read_b32 v170, a129\n" \
  "v_mfma_f32_16x16x32_bf16 v[104:107], a[36:39], v[188:191], v[104:107]\n" \
  "buffer_load_dwordx4 a[68:71], v17, s[36:39], s14 offen sc1\n" \
  "v_accvgpr_read_b32 v174, a130\n" \
  "v_mfma_f32_16x16x32_bf16 v[108:111], a[36:39], v[192:195], v[108:111]\n" \
  "v_accvgpr_read_b32 v178, a131\n" \
  "v_accvgpr_read_b32 v167, a132\n" \
  "s_waitcnt lgkmcnt(7)\n" \
  "v_mfma_f32_16x16x32_bf16 v[116:119], a[40:43], v[192:195], v[116:119]\n" \
  "v_accvgpr_read_b32 v171, a133\n" \
  "v_accvgpr_read_b32 v175, a134\n" \
  "v_mfma_f32_16x16x32_bf16 v[112:115], a[40:43], v[188:191], v[112:115]\n" \
  "s_mov_b32 s14, 0x830000\n" \
  "buffer_load_dwordx4 a[72:75], v14, s[36:39], s14 offen sc1\n" \
  "v_mfma_f32_16x16x32_bf16 v[60:63], a[40:43], v[184:187], v[60:63]\n" \
  "v_accvgpr_read_b32 v179, a135\n" \
  "v_max3_u32 v2, v164, v165, v166\n" \
  "v_mfma_f32_16x16x32_bf16 v[56:59], a[40:43], v[180:183], v[56:59]\n" \
  "v_max_u32 v2, v2, v167\n" \
  "v_cmp_ne_u32 vcc, -1, v2\n" \
  "v_mfma_f32_16x16x32_bf16 v[64:67], a[44:47], v[180:183], v[64:67]\n" \
  "s_cmp_eq_u64 vcc, -1\n" \
  "s_cbranch_scc0 LS4_slow%=\n" \
  "LS4_ok%=:\n" \
  "v_mfma_f32_16x16x32_bf16 v[68:71], a[44:47], v[184:187], v[68:71]\n" \
  "buffer_load_dwordx4 a[76:79], v15, s[36:39], s14 offen sc1\n" \
  "v_mfma_f32_16x16x32_bf16 v[120:123], a[44:47], v[188:191], v[120:123]\n" \
  "v_mfma_f32_16x16x32_bf16 v[124:127], a[44:47], v[192:195], v[124:127]\n" \
  "v_mfma_f32_16x16x32_bf16 v[132:135], a[48:51], v[192:195], v[132:135]\n" \
  "v_mfma_f32_16x16x32_bf16 v[128:131], a[48:51], v[188:191], v[128:131]\n" \
  "buffer_load_dwordx4 a[80:83], v16, s[36:39], s14 offen sc1\n" \
  "v_mfma_f32_16x16x32_bf16 v[76:79], a[48:51], v[184:187], v[76:79]\n" \
  "v_mfma_f32_16x16x32_bf16 v[72:75], a[48:51], v[180:183], v[72:75]\n" \
  "v_mfma_f32_16x16x32_bf16 v[80:83], a[52:55], v[180:183], v[80:83]\n" \
  "v_mfma_f32_16x16x32_bf16 v[84:87], a[52:55], v[184:187], v[84:87]\n" \
  "buffer_load_dwordx4 a[84:87], v17, s[36:39], s14 offen sc1\n" \
  "v_mfma_f32_16x16x32_bf16 v[136:139], a[52:55], v[188:191], v[136:139]\n" \
  "v_mfma_f32_16x16x32_bf16 v[140:143], a[52:55], v[192:195], v[140:143]\n" \
  "v_mfma_f32_16x16x32_bf16 v[156:159], v[192:195], v[192:195], v[156:159]\n" \
  "v_mfma_f32_16x16x32_bf16 v[152:155], v[188:191], v[188:191], v[152:155]\n" \
  "s_mov_b32 s14, 0x838000\n" \
  "buffer_load_dwordx4 a[88:91], v14, s[36:39], s14 offen sc1\n" \
  "v_mfma_f32_16x16x32_bf16 v[148:151], v[184:187], v[184:187], v[148:151]\n" \
  "v_mfma_f32_16x16x32_bf16 v[144:147], v[180:183], v[180:183], v[144:147]\n" \
  "s_waitcnt lgkmcnt(4)\n" \
  "v_mfma_f32_16x16x32_bf16 v[32:35], a[0:3], v[164:167], v[32:35]\n" \
  "ds_read_b128 a[28:31], v22 offset:35840\n" \
  "ds_read_b128 a[32:35], v22 offset:36864\n" \
  "v_mfma_f32_16x16x32_bf16 v[36:39], a[0:3], v[168:171], v[36:39]\n" \
  "buffer_load_dwordx4 a[92:95], v15, s[36:39], s14 offen sc1\n" \
  "ds_read_b128 a[36:39], v22 offset:37888\n" \
  "v_mfma_f32_16x16x32_bf16 v[88:91], a[0:3], v[172:175], v[88:91]\n" \
  "ds_read_b128 a[40:43], v22 offset:38912\n" \
  "ds_read_b128 a[44:47], v22 offset:39936\n" \
  "v_mfma_f32_16x16x32_bf16 v[92:95], a[0:3], v[176:179], v[92:95]\n" \
  "ds_read_b128 a[48:51], v22 offset:40960\n" \
  "ds_read_b128 a[52:55], v22 offset:41984\n" \
  "v_mfma_f32_16x16x32_bf16 v[100:103], a[4:7], v[176:179], v[100:103]\n" \
  "s_waitcnt vmcnt(6)\n" \
  "v_accvgpr_read_b32 v180, a56\n" \
  "v_mfma_f32_16x16x32_bf16 v[96:99], a[4:7], v[172:175], v[96:99]\n" \
  "buffer_load_dwordx4 a[96:99], v16, s[36:39], s14 offen sc1\n" \
  "v_accvgpr_read_b32 v184, a57\n" \
  "v_mfma_f32_16x16x32_bf16 v[44:47], a[4:7], v[168:171], v[44:47]\n" \
  "v_accvgpr_read_b32 v188, a58\n" \
  "v_accvgpr_read_b32 v192, a59\n" \
  "v_mfma_f32_16x16x32_bf16 v[40:43], a[4:7], v[164:167], v[40:43]\n" \
  "v_accvgpr_read_b32 v181, a60\n" \
  "v_accvgpr_read_b32 v185, a61\n" \
  "v_mfma_f32_16x16x32_bf16 v[48:51], a[8:11], v[164:167], v[48:51]\n" \
  "v_accvgpr_read_b32 v189, a62\n" \
  "v_accvgpr_read_b32 v193, a63\n" \
  "v_mfma_f32_16x16x32_bf16 v[52:55], a[8:11], v[168:171], v[52:55]\n" \
  "buffer_load_dwordx4 a[100:103], v17, s[36:39], s14 offen sc1\n" \
  "v_accvgpr_read_b32 v182, a64\n" \
  "v_mfma_f32_16x16x32_bf16 v[104:107], a[8:11], v[172:175], v[104:107]\n" \
  "v_accvgpr_read_b32 v186, a65\n" \
  "v_accvgpr_read_b32 v190, a66\n" \
  "v_mfma_f32_16x16x32_bf16 v[108:111], a[8:11], v[176:179], v[108:111]\n" \
  "v_accvgpr_read_b32 v194, a67\n" \
  "v_accvgpr_read_b32 v183, a68\n" \
  "s_waitcnt lgkmcnt(7)\n" \
  "v_mfma_f32_16x16x32_bf16 v[116:119], a[12:15], v[176:179], v[116:119]\n" \
  "v_accvgpr_read_b32 v187, a69\n" \
  "v_accvgpr_read_b32 v191, a70\n" \
  "v_mfma_f32_16x16x32_bf16 v[112:115], a[12:15], v[172:175], v[112:115]\n" \
  "s_mov_b32 s14, 0x900000\n" \
  "buffer_load_dwordx4 a[104:107], v14, s[36:39], s14 offen sc1\n" \
  "v_mfma_f32_16x16x32_bf16 v[60:63], a[12:15], v[168:171], v[60:63]\n" \
  "v_accvgpr_read_b32 v195, a71\n" \
  "v_max3_u32 v2, v180, v181, v182\n" \
  "v_mfma_f32_16x16x32_bf16 v[56:59], a[12:15], v[164:167], v[56:59]\n" \
  "v_max_u32 v2, v2, v183\n" \
  "v_cmp_ne_u32 vcc, -1, v2\n" \
  "v_mfma_f32_16x16x32_bf16 v[64:67], a[16:19], v[164:167], v[64:67]\n" \
  "s_cmp_eq_u64 vcc, -1\n" \
  "s_cbranch_scc0 LS5_slow%=\n" \
  "LS5_ok%=:\n" \
  "v_mfma_f32_16x16x32_bf16 v[68:71], a[16:19], v[168:171], v[68:71]\n" \
  "buffer_load_dwordx4 a[108:111], v15, s[36:39], s14 offen sc1\n" \
  "buffer_load_dwordx2 v[26:27], v6, s[36:39], 0 offen sc1\n" \
  "v_mfma_f32_16x16x32_bf16 v[120:123], a[16:19], v[172:175], v[120:123]\n" \
  "v_mfma_f32_16x16x32_bf16 v[124:127], a[16:19], v[176:179], v[124:127]\n" \
  "v_mfma_f32_16x16x32_bf16 v[132:135], a[20:23], v[176:179], v[132:135]\n" \
  "v_mfma_f32_16x16x32_bf16 v[128:131], a[20:23], v[172:175], v[128:131]\n" \
  "buffer_load_dwordx4 a[112:115], v16, s[36:39], s14 offen sc1\n" \
  "v_mfma_f32_16x16x32_bf16 v[76:79], a[20:23], v[168:171], v[76:79]\n" \
  "v_mfma_f32_16x16x32_bf16 v[72:75], a[20:23], v[164:167], v[72:75]\n" \
  "v_mfma_f32_16x16x32_bf16 v[80:83], a[24:27], v[164:167], v[80:83]\n" \
  "v_mfma_f32_16x16x32_bf16 v[84:87], a[24:27], v[168:171], v[84:87]\n" \
  "buffer_load_dwordx4 a[116:119], v17, s[36:39], s14 offen sc1\n" \
  "v_mfma_f32_16x16x32_bf16 v[136:139], a[24:27], v[172:175], v[136:139]\n" \
  "v_mfma_f32_16x16x32_bf16 v[140:143], a[24:27], v[176:179], v[140:143]\n" \
  "v_mfma_f32_16x16x32_bf16 v[156:159], v[176:179], v[176:179], v[156:159]\n" \
  "v_mfma_f32_16x16x32_bf16 v[152:155], v[172:175], v[172:175], v[152:155]\n" \
  "s_mov_b32 s14, 0x908000\n" \
  "buffer_load_dwordx4 a[120:123], v14, s[36:39], s14 offen sc1\n" \
  "v_mfma_f32_16x16x32_bf16 v[148:151], v[168:171], v[168:171], v[148:151]\n" \
  "v_mfma_f32_16x16x32_bf16 v[144:147], v[164:167], v[164:167], v[144:147]\n" \
  "s_waitcnt lgkmcnt(4)\n" \
  "v_mfma_f32_16x16x32_bf16 v[32:35], a[28:31], v[180:183], v[32:35]\n" \
  "ds_read_b128 a[0:3], v22 offset:43008\n" \
  "ds_read_b128 a[4:7], v22 offset:44032\n" \
  "v_mfma_f32_16x16x32_bf16 v[36:39], a[28:31], v[184:187], v[36:39]\n" \
  "buffer_load_dwordx4 a[124:127], v15, s[36:39], s14 offen sc1\n" \
  "ds_read_b128 a[8:11], v22 offset:45056\n" \
  "v_mfma_f32_16x16x32_bf16 v[88:91], a[28:31], v[188:191], v[88:91]\n" \
  "ds_read_b128 a[12:15], v22 offset:46080\n" \
  "ds_read_b128 a[16:19], v22 offset:47104\n" \
  "v_mfma_f32_16x16x32_bf16 v[92:95], a[28:31], v[192:195], v[92:95]\n" \
  "ds_read_b128 a[20:23], v22 offset:48128\n" \
  "ds_read_b128 a[24:27], v22 offset:49152\n" \
  "v_mfma_f32_16x16x32_bf16 v[100:103], a[32:35], v[192:195], v[100:103]\n" \
  "s_waitcnt vmcnt(11)\n" \
  "v_accvgpr_read_b32 v164, a72\n" \
  "v_mfma_f32_16x16x32_bf16 v[96:99], a[32:35], v[188:191], v[96:99]\n" \
  "buffer_load_dwordx4 a[128:131], v16, s[36:39], s14 offen sc1\n" \
  "v_accvgpr_read_b32 v168, a73\n" \
  "v_mfma_f32_16x16x32_bf16 v[44:47], a[32:35], v[184:187], v[44:47]\n" \
  "v_accvgpr_read_b32 v172, a74\n" \
  "v_accvgpr_read_b32 v176, a75\n" \
  "v_mfma_f32_16x16x32_bf16 v[40:43], a[32:35], v[180:183], v[40:43]\n" \
  "v_accvgpr_read_b32 v165, a76\n" \
  "v_accvgpr_read_b32 v169, a77\n" \
  "v_mfma_f32_16x16x32_bf16 v[48:51], a[36:39], v[180:183], v[48:51]\n" \
  "v_accvgpr_read_b32 v173, a78\n" \
  "v_accvgpr_read_b32 v177, a79\n" \
  "v_mfma_f32_16x16x32_bf16 v[52:55], a[36:39], v[184:187], v[52:55]\n" \
  "buffer_load_dwordx4 a[132:135], v17, s[36:39], s14 offen sc1\n" \
  "v_accvgpr_read_b32 v166, a80\n" \
  "v_mfma_f32_16x16x32_bf16 v[104:107], a[36:39], v[188:191], v[104:107]\n" \
  "v_accvgpr_read_b32 v170, a81\n" \
  "v_accvgpr_read_b32 v174, a82\n" \
  "v_mfma_f32_16x16x32_bf16 v[108:111], a[36:39], v[192:195], v[108:111]\n" \
  "v_accvgpr_read_b32 v178, a83\n" \
  "v_accvgpr_read_b32 v167, a84\n" \
  "s_waitcnt lgkmcnt(7)\n" \
  "v_mfma_f32_16x16x32_bf16 v[116:119], a[40:43], v[192:195], v[116:119]\n" \
  "s_mov_b32 s14, 0x910000\n" \
  "buffer_load_dwordx4 a[56:59], v14, s[36:39], s14 offen sc1\n" \
  "v_mfma_f32_16x16x32_bf16 v[112:115], a[40:43], v[188:191], v[112:115]\n" \
  "v_accvgpr_read_b32 v171, a85\n" \
  "v_accvgpr_read_b32 v175, a86\n" \
  "v_mfma_f32_16x16x32_bf16 v[60:63], a[40:43], v[184:187], v[60:63]\n" \
  "v_accvgpr_read_b32 v179, a87\n" \
  "v_max3_u32 v2, v164, v165, v166\n" \
  "v_mfma_f32_16x16x32_bf16 v[56:59], a[40:43], v[180:183], v[56:59]\n" \
  "v_max_u32 v2, v2, v167\n" \
  "v_cmp_ne_u32 vcc, -1, v2\n" \
  "v_mfma_f32_16x16x32_bf16 v[64:67], a[44:47], v[180:183], v[64:67]\n" \
  "buffer_load_dwordx4 a[60:63], v15, s[36:39], s14 offen sc1\n" \
  "s_cmp_eq_u64 vcc, -1\n" \
  "v_mfma_f32_16x16x32_bf16 v[68:71], a[44:47], v[184:187], v[68:71]\n" \
  "s_cbranch_scc0 LS6_slow%=\n" \
  "LS6_ok%=:\n" \
  "s_waitcnt vmcnt(8)\n" \
  "v_mfma_f32_16x16x32_bf16 v[120:123], a[44:47], v[188:191], v[120:123]\n" \
  "v_cmp_eq_u64 vcc, s[48:49], v[26:27]\n" \
  "s_nop 0\n" \
  "s_cmp_eq_u64 vcc, -1\n" \
  "v_mfma_f32_16x16x32_bf16 v[124:127], a[44:47], v[192:195], v[124:127]\n" \
  "s_cbranch_scc0 LFD_slow%=\n" \
  "LFD_ok%=:\n" \
  "v_mfma_f32_16x16x32_bf16 v[132:135], a[48:51], v[192:195], v[132:135]\n" \
  "buffer_load_dwordx4 a[64:67], v16, s[36:39], s14 offen sc1\n" \
  "v_mfma_f32_16x16x32_bf16 v[128:131], a[48:51], v[188:191], v[128:131]\n" \
  "v_mfma_f32_16x16x32_bf16 v[76:79], a[48:51], v[184:187], v[76:79]\n" \
  "v_mfma_f32_16x16x32_bf16 v[72:75], a[48:51], v[180:183], v[72:75]\n" \
  "v_mfma_f32_16x16x32_bf16 v[80:83], a[52:55], v[180:183], v[80:83]\n" \
  "buffer_load_dwordx4 a[68:71], v17, s[36:39], s14 offen sc1\n" \
  "v_mfma_f32_16x16x32_bf16 v[84:87], a[52:55], v[184:187], v[84:87]\n" \
  "v_mfma_f32_16x16x32_bf16 v[136:139], a[52:55], v[188:191], v[136:139]\n" \
  "v_mfma_f32_16x16x32_bf16 v[140:143], a[52:55], v[192:195], v[140:143]\n" \
  "v_mfma_f32_16x16x32_bf16 v[156:159], v[192:195], v[192:195], v[156:159]\n" \
  "s_mov_b32 s14, 0x918000\n" \
  "buffer_load_dwordx4 a[72:75], v14, s[36:39], s14 offen sc1\n" \
  "v_mfma_f32_16x16x32_bf16 v[152:155], v[188:191], v[188:191], v[152:155]\n" \
  "v_mfma_f32_16x16x32_bf16 v[148:151], v[184:187], v[184:187], v[148:151]\n" \
  "v_mfma_f32_16x16x32_bf16 v[144:147], v[180:183], v[180:183], v[144:147]\n" \
  "s_waitcnt lgkmcnt(4)\n" \
  "v_mfma_f32_16x16x32_bf16 v[32:35], a[0:3], v[164:167], v[32:35]\n" \
  "buffer_load_dwordx4 a[76:79], v15, s[36:39], s14 offen sc1\n" \
  "ds_read_b128 a[28:31], v22 offset:50176\n" \
  "v_mfma_f32_16x16x32_bf16 v[36:39], a[0:3], v[168:171], v[36:39]\n" \
  "ds_read_b128 a[32:35], v22 offset:51200\n" \
  "ds_read_b128 a[36:39], v22 offset:52224\n" \
  "v_mfma_f32_16x16x32_bf16 v[88:91], a[0:3], v[172:175], v[88:91]\n" \
  "ds_read_b128 a[40:43], v22 offset:53248\n" \
  "ds_read_b128 a[44:47], v22 offset:54272\n" \
  "v_mfma_f32_16x16x32_bf16 v[92:95], a[0:3], v[176:179], v[92:95]\n" \
  "ds_read_b128 a[48:51], v22 offset:55296\n" \
  "ds_read_b128 a[52:55], v22 offset:56320\n" \
  "v_mfma_f32_16x16x32_bf16 v[100:103], a[4:7], v[176:179], v[100:103]\n" \
  "buffer_load_dwordx4 a[80:83], v16, s[36:39], s14 offen sc1\n" \
  "s_waitcnt vmcnt(16)\n" \
  "v_mfma_f32_16x16x32_bf16 v[96:99], a[4:7], v[172:175], v[96:99]\n" \
  "v_accvgpr_read_b32 v180, a88\n" \
  "v_accvgpr_read_b32 v184, a89\n" \
  "v_mfma_f32_16x16x32_bf16 v[44:47], a[4:7], v[168:171], v[44:47]\n" \
  "v_accvgpr_read_b32 v188, a90\n" \
  "v_accvgpr_read_b32 v192, a91\n" \
  "v_mfma_f32_16x16x32_bf16 v[40:43], a[4:7], v[164:167], v[40:43]\n" \
  "v_accvgpr_read_b32 v181, a92\n" \
  "v_accvgpr_read_b32 v185, a93\n" \
  "v_mfma_f32_16x16x32_bf16 v[48:51], a[8:11], v[164:167], v[48:51]\n" \
  "buffer_load_dwordx4 a[84:87], v17, s[36:39], s14 offen sc1\n" \
  "v_accvgpr_read_b32 v189, a94\n" \
  "v_mfma_f32_16x16x32_bf16 v[52:55], a[8:11], v[168:171], v[52:55]\n" \
  "v_accvgpr_read_b32 v193, a95\n" \
  "v_accvgpr_read_b32 v182, a96\n" \
  "v_mfma_f32_16x16x32_bf16 v[104:107], a[8:11], v[172:175], v[104:107]\n" \
  "v_accvgpr_read_b32 v186, a97\n" \
  "v_accvgpr_read_b32 v190, a98\n" \
  "v_mfma_f32_16x16x32_bf16 v[108:111], a[8:11], v[176:179], v[108:111]\n" \
  "v_accvgpr_read_b32 v194, a99\n" \
  "v_accvgpr_read_b32 v183, a100\n" \
  "s_waitcnt lgkmcnt(7)\n" \
  "v_mfma_f32_16x16x32_bf16 v[116:119], a[12:15], v[176:179], v[116:119]\n" \
  "buffer_load_dwordx4 a[136:139], v28, s[40:43], 0 offen\n" \
  "v_accvgpr_read_b32 v187, a101\n" \
  "v_mfma_f32_16x16x32_bf16 v[112:115], a[12:15], v[172:175], v[112:115]\n" \
  "v_accvgpr_read_b32 v191, a102\n" \
  "v_accvgpr_read_b32 v195, a103\n" \
  "v_mfma_f32_16x16x32_bf16 v[60:63], a[12:15], v[168:171], v[60:63]\n" \
  "v_max3_u32 v2, v180, v181, v182\n" \
  "v_max_u32 v2, v2, v183\n" \
  "v_mfma_f32_16x16x32_bf16 v[56:59], a[12:15], v[164:167], v[56:59]\n" \
  "v_cmp_ne_u32 vcc, -1, v2\n" \
  "s_nop 0\n" \
  "s_cmp_eq_u64 vcc, -1\n" \
  "v_mfma_f32_16x16x32_bf16 v[64:67], a[16:19], v[164:167], v[64:67]\n" \
  "s_cbranch_scc0 LS7_slow%=\n" \
  "LS7_ok%=:\n" \
  "buffer_load_dwordx2 v[26:27], v8, s[36:39], 0 offen sc1\n" \
  "v_mfma_f32_16x16x32_bf16 v[68:71], a[16:19], v[168:171], v[68:71]\n" \
  "v_mfma_f32_16x16x32_bf16 v[120:123], a[16:19], v[172:175], v[120:123]\n" \
  "v_mfma_f32_16x16x32_bf16 v[124:127], a[16:19], v[176:179], v[124:127]\n" \
  "v_mfma_f32_16x16x32_bf16 v[132:135], a[20:23], v[176:179], v[132:135]\n" \
  "v_mfma_f32_16x16x32_bf16 v[128:131], a[20:23], v[172:175], v[128:131]\n" \
  "v_mfma_f32_16x16x32_bf16 v[76:79], a[20:23], v[168:171], v[76:79]\n" \
  "v_mfma_f32_16x16x32_bf16 v[72:75], a[20:23], v[164:167], v[72:75]\n" \
  "v_mfma_f32_16x16x32_bf16 v[80:83], a[24:27], v[164:167], v[80:83]\n" \
  "v_mfma_f32_16x16x32_bf16 v[84:87], a[24:27], v[168:171], v[84:87]\n" \
  "v_mfma_f32_16x16x32_bf16 v[136:139], a[24:27], v[172:175], v[136:139]\n" \
  "s_mov_b32 s14, 0x920000\n" \
  "buffer_load_dwordx4 a[88:91], v14, s[36:39], s14 offen sc1\n" \
  "v_mfma_f32_16x16x32_bf16 v[140:143], a[24:27], v[176:179], v[140:143]\n" \
  "v_mfma_f32_16x16x32_bf16 v[156:159], v[176:179], v[176:179], v[156:159]\n" \
  "v_mfma_f32_16x16x32_bf16 v[152:155], v[172:175], v[172:175], v[152:155]\n" \
  "v_mfma_f32_16x16x32_bf16 v[148:151], v[168:171], v[168:171], v[148:151]\n" \
  "buffer_load_dwordx4 a[92:95], v15, s[36:39], s14 offen sc1\n" \
  "v_mfma_f32_16x16x32_bf16 v[144:147], v[164:167], v[164:167], v[144:147]\n" \
  "s_waitcnt vmcnt(33)\n" \
  "s_barrier\n" \
  "s_waitcnt lgkmcnt(4)\n" \
  "v_mfma_f32_16x16x32_bf16 v[32:35], a[28:31], v[180:183], v[32:35]\n" \
  "ds_read_b128 a[0:3], v23\n" \
  "ds_read_b128 a[4:7], v23 offset:1024\n" \
  "v_mfma_f32_16x16x32_bf16 v[36:39], a[28:31], v[184:187], v[36:39]\n" \
  "buffer_load_dwordx4 a[96:99], v16, s[36:39], s14 offen sc1\n" \
  "ds_read_b128 a[8:11], v23 offset:2048\n" \
  "v_mfma_f32_16x16x32_bf16 v[88:91], a[28:31], v[188:191], v[88:91]\n" \
  "ds_read_b128 a[12:15], v23 offset:3072\n" \
  "ds_read_b128 a[16:19], v23 offset:4096\n" \
  "v_mfma_f32_16x16x32_bf16 v[92:95], a[28:31], v[192:195], v[92:95]\n" \
  "ds_read_b128 a[20:23], v23 offset:5120\n" \
  "ds_read_b128 a[24:27], v23 offset:6144\n" \
  "v_mfma_f32_16x16x32_bf16 v[100:103], a[32:35], v[192:195], v[100:103]\n" \
  "s_waitcnt vmcnt(17)\n" \
  "v_accvgpr_read_b32 v164, a104\n" \
  "v_mfma_f32_16x16x32_bf16 v[96:99], a[32:35], v[188:191], v[96:99]\n" \
  "buffer_load_dwordx4 a[100:103], v17, s[36:39], s14 offen sc1\n" \
  "v_accvgpr_read_b32 v168, a105\n" \
  "v_mfma_f32_16x16x32_bf16 v[44:47], a[32:35], v[184:187], v[44:47]\n" \
  "v_accvgpr_read_b32 v172, a106\n" \
  "v_accvgpr_read_b32 v176, a107\n" \
  "v_mfma_f32_16x16x32_bf16 v[40:43], a[32:35], v[180:183], v[40:43]\n" \
  "v_accvgpr_read_b32 v165, a108\n" \
  "v_accvgpr_read_b32 v169, a109\n" \
  "v_mfma_f32_16x16x32_bf16 v[48:51], a[36:39], v[180:183], v[48:51]\n" \
  "v_accvgpr_read_b32 v173, a110\n" \
  "v_accvgpr_read_b32 v177, a111\n" \
  "v_mfma_f32_16x16x32_bf16 v[52:55], a[36:39], v[184:187], v[52:55]\n" \
  "buffer_load_dwordx4 a[140:143], v29, s[40:43], 0 offen\n" \
  "v_accvgpr_read_b32 v166, a112\n" \
  "v_mfma_f32_16x16x32_bf16 v[104:107], a[36:39], v[188:191], v[104:107]\n" \
  "v_accvgpr_read_b32 v170, a113\n" \
  "v_accvgpr_read_b32 v174, a114\n" \
  "v_mfma_f32_16x16x32_bf16 v[108:111], a[36:39], v[192:195], v[108:111]\n" \
  "v_accvgpr_read_b32 v178, a115\n" \
  "v_accvgpr_read_b32 v167, a116\n" \
  "s_waitcnt lgkmcnt(7)\n" \
  "v_mfma_f32_16x16x32_bf16 v[116:119], a[40:43], v[192:195], v[116:119]\n" \
  "v_accvgpr_read_b32 v171, a117\n" \
  "v_accvgpr_read_b32 v175, a118\n" \
  "v_mfma_f32_16x16x32_bf16 v[112:115], a[40:43], v[188:191], v[112:115]\n" \
  "v_accvgpr_read_b32 v179, a119\n" \
  "v_max3_u32 v2, v164, v165, v166\n" \
  "v_mfma_f32_16x16x32_bf16 v[60:63], a[40:43], v[184:187], v[60:63]\n" \
  "v_max_u32 v2, v2, v167\n" \
  "v_cmp_ne_u32 vcc, -1, v2\n" \
  "v_mfma_f32_16x16x32_bf16 v[56:59], a[40:43], v[180:183], v[56:59]\n" \
  "s_mov_b32 s15, 0x1800000\n" \
  "buffer_load_dwordx4 a[144:147], v24, s[36:39], s15 offen sc1\n" \
  "v_mfma_f32_16x16x32_bf16 v[64:67], a[44:47], v[180:183], v[64:67]\n" \
  "s_cmp_eq_u64 vcc, -1\n" \
  "s_cbranch_scc0 LS8_slow%=\n" \
  "LS8_ok%=:\n" \
  "v_mfma_f32_16x16x32_bf16 v[68:71], a[44:47], v[184:187], v[68:71]\n" \
  "v_mfma_f32_16x16x32_bf16 v[120:123], a[44:47], v[188:191], v[120:123]\n" \
  "v_mfma_f32_16x16x32_bf16 v[124:127], a[44:47], v[192:195], v[124:127]\n" \
  "s_mov_b32 s14, 0x928000\n" \
  "buffer_load_dwordx4 a[104:107], v14, s[36:39], s14 offen sc1\n" \
  "v_mfma_f32_16x16x32_bf16 v[132:135], a[48:51], v[192:195], v[132:135]\n" \
  "v_mfma_f32_16x16x32_bf16 v[128:131], a[48:51], v[188:191], v[128:131]\n" \
  "v_mfma_f32_16x16x32_bf16 v[76:79], a[48:51], v[184:187], v[76:79]\n" \
  "v_mfma_f32_16x16x32_bf16 v[72:75], a[48:51], v[180:183], v[72:75]\n" \
  "buffer_load_dwordx4 a[108:111], v15, s[36:39], s14 offen sc1\n" \
  "v_mfma_f32_16x16x32_bf16 v[80:83], a[52:55], v[180:183], v[80:83]\n" \
  "v_mfma_f32_16x16x32_bf16 v[84:87], a[52:55], v[184:187], v[84:87]\n" \
  "v_mfma_f32_16x16x32_bf16 v[136:139], a[52:55], v[188:191], v[136:139]\n" \
  "v_mfma_f32_16x16x32_bf16 v[140:143], a[52:55], v[192:195], v[140:143]\n" \
  "buffer_load_dwordx4 a[112:115], v16, s[36:39], s14 offen sc1\n" \
  "v_mfma_f32_16x16x32_bf16 v[156:159], v[192:195], v[192:195], v[156:159]\n" \
  "v_mfma_f32_16x16x32_bf16 v[152:155], v[188:191], v[188:191], v[152:155]\n" \
  "v_mfma_f32_16x16x32_bf16 v[148:151], v[184:187], v[184:187], v[148:151]\n" \
  "v_mfma_f32_16x16x32_bf16 v[144:147], v[180:183], v[180:183], v[144:147]\n" \
  "buffer_load_dwordx4 a[116:119], v17, s[36:39], s14 offen sc1\n" \
  "s_waitcnt lgkmcnt(4)\n" \
  "v_mfma_f32_16x16x32_bf16 v[32:35], a[0:3], v[164:167], v[32:35]\n" \
  "ds_read_b128 a[28:31], v23 offset:7168\n" \
  "ds_read_b128 a[32:35], v23 offset:8192\n" \
  "v_mfma_f32_16x16x32_bf16 v[36:39], a[0:3], v[168:171], v[36:39]\n" \
  "ds_read_b128 a[36:39], v23 offset:9216\n" \
  "ds_read_b128 a[40:43], v23 offset:10240\n" \
  "v_mfma_f32_16x16x32_bf16 v[88:91], a[0:3], v[172:175], v[88:91]\n" \
  "ds_read_b128 a[44:47], v23 offset:11264\n" \
  "ds_read_b128 a[48:51], v23 offset:12288\n" \
  "v_mfma_f32_16x16x32_bf16 v[92:95], a[0:3], v[176:179], v[92:95]\n" \
  "buffer_load_dwordx4 a[148:151], v24, s[36:39], s15 offen offset:256 sc1\n" \
  "ds_read_b128 a[52:55], v23 offset:13312\n" \
  "v_mfma_f32_16x16x32_bf16 v[100:103], a[4:7], v[176:179], v[100:103]\n" \
  "s_waitcnt vmcnt(21)\n" \
  "v_accvgpr_read_b32 v180, a120\n" \
  "v_mfma_f32_16x16x32_bf16 v[96:99], a[4:7], v[172:175], v[96:99]\n" \
  "v_accvgpr_read_b32 v184, a121\n" \
  "v_accvgpr_read_b32 v188, a122\n" \
  "v_mfma_f32_16x16x32_bf16 v[44:47], a[4:7], v[168:171], v[44:47]\n" \
  "v_accvgpr_read_b32 v192, a123\n" \
  "v_accvgpr_read_b32 v181, a124\n" \
  "v_mfma_f32_16x16x32_bf16 v[40:43], a[4:7], v[164:167], v[40:43]\n" \
  "buffer_load_dwordx4 a[152:155], v24, s[36:39], s15 offen offset:512 sc1\n" \
  "v_accvgpr_read_b32 v185, a125\n" \
  "v_mfma_f32_16x16x32_bf16 v[48:51], a[8:11], v[164:167], v[48:51]\n" \
  "v_accvgpr_read_b32 v189, a126\n" \
  "v_accvgpr_read_b32 v193, a127\n" \
  "v_mfma_f32_16x16x32_bf16 v[52:55], a[8:11], v[168:171], v[52:55]\n" \
  "v_accvgpr_read_b32 v182, a128\n" \
  "v_accvgpr_read_b32 v186, a129\n" \
  "v_mfma_f32_16x16x32_bf16 v[104:107], a[8:11], v[172:175], v[104:107]\n" \
  "v_accvgpr_read_b32 v190, a130\n" \
  "v_accvgpr_read_b32 v194, a131\n" \
  "v_mfma_f32_16x16x32_bf16 v[108:111], a[8:11], v[176:179], v[108:111]\n" \
  "buffer_load_dwordx4 a[156:159], v24, s[36:39], s15 offen offset:768 sc1\n" \
  "v_accvgpr_read_b32 v183, a132\n" \
  "s_waitcnt lgkmcnt(7)\n" \
  "v_mfma_f32_16x16x32_bf16 v[116:119], a[12:15], v[176:179], v[116:119]\n" \
  "v_accvgpr_read_b32 v187, a133\n" \
  "v_accvgpr_read_b32 v191, a134\n" \
  "v_mfma_f32_16x16x32_bf16 v[112:115], a[12:15], v[172:175], v[112:115]\n" \
  "v_accvgpr_read_b32 v195, a135\n" \
  "v_max3_u32 v2, v180, v181, v182\n" \
  "v_mfma_f32_16x16x32_bf16 v[60:63], a[12:15], v[168:171], v[60:63]\n" \
  "v_max_u32 v2, v2, v183\n" \
  "v_cmp_ne_u32 vcc, -1, v2\n" \
  "v_mfma_f32_16x16x32_bf16 v[56:59], a[12:15], v[164:167], v[56:59]\n" \
  "s_mov_b32 s15, 0x1801000\n" \
  "buffer_load_dwordx4 a[160:163], v24, s[36:39], s15 offen sc1\n" \
  "v_mfma_f32_16x16x32_bf16 v[64:67], a[16:19], v[164:167], v[64:67]\n" \
  "s_cmp_eq_u64 vcc, -1\n" \
  "s_cbranch_scc0 LS9_slow%=\n" \
  "LS9_ok%=:\n" \
  "v_mfma_f32_16x16x32_bf16 v[68:71], a[16:19], v[168:171], v[68:71]\n" \
  "s_waitcnt vmcnt(14)\n" \
  "v_cmp_eq_u64 vcc, s[48:49], v[26:27]\n" \
  "v_mfma_f32_16x16x32_bf16 v[120:123], a[16:19], v[172:175], v[120:123]\n" \
  "s_cmp_eq_u64 vcc, -1\n" \
  "s_cbranch_scc0 LF2_slow%=\n" \
  "LF2_ok%=:\n" \
  "v_mfma_f32_16x16x32_bf16 v[124:127], a[16:19], v[176:179], v[124:127]\n" \
  "s_mov_b32 s14, 0x930000\n" \
  "buffer_load_dwordx4 a[120:123], v14, s[36:39], s14 offen sc1\n" \
  "v_mfma_f32_16x16x32_bf16 v[132:135], a[20:23], v[176:179], v[132:135]\n" \
  "v_mfma_f32_16x16x32_bf16 v[128:131], a[20:23], v[172:175], v[128:131]\n" \
  "v_mfma_f32_16x16x32_bf16 v[76:79], a[20:23], v[168:171], v[76:79]\n" \
  "v_mfma_f32_16x16x32_bf16 v[72:75], a[20:23], v[164:167], v[72:75]\n" \
  "buffer_load_dwordx4 a[124:127], v15, s[36:39], s14 offen sc1\n" \
  "v_mfma_f32_16x16x32_bf16 v[80:83], a[24:27], v[164:167], v[80:83]\n" \
  "v_mfma_f32_16x16x32_bf16 v[84:87], a[24:27], v[168:171], v[84:87]\n" \
  "v_mfma_f32_16x16x32_bf16 v[136:139], a[24:27], v[172:175], v[136:139]\n" \
  "v_mfma_f32_16x16x32_bf16 v[140:143], a[24:27], v[176:179], v[140:143]\n" \
  "buffer_load_dwordx4 a[128:131], v16, s[36:39], s14 offen sc1\n" \
  "v_mfma_f32_16x16x32_bf16 v[156:159], v[176:179], v[176:179], v[156:159]\n" \
  "v_mfma_f32_16x16x32_bf16 v[152:155], v[172:175], v[172:175], v[152:155]\n" \
  "v_mfma_f32_16x16x32_bf16 v[148:151], v[168:171], v[168:171], v[148:151]\n" \
  "v_mfma_f32_16x16x32_bf16 v[144:147], v[164:167], v[164:167], v[144:147]\n" \
  "buffer_load_dwordx4 a[132:135], v17, s[36:39], s14 offen sc1\n" \
  "s_waitcnt lgkmcnt(4)\n" \
  "v_mfma_f32_16x16x32_bf16 v[32:35], a[28:31], v[180:183], v[32:35]\n" \
  "ds_read_b128 a[0:3], v23 offset:14336\n" \
  "ds_read_b128 a[4:7], v23 offset:15360\n" \
  "v_mfma_f32_16x16x32_bf16 v[36:39], a[28:31], v[184:187], v[36:39]\n" \
  "ds_read_b128 a[8:11], v23 offset:16384\n" \
  "ds_read_b128 a[12:15], v23 offset:17408\n" \
  "v_mfma_f32_16x16x32_bf16 v[88:91], a[28:31], v[188:191], v[88:91]\n" \
  "ds_read_b128 a[16:19], v23 offset:18432\n" \
  "ds_read_b128 a[20:23], v23 offset:19456\n" \
  "v_mfma_f32_16x16x32_bf16 v[92:95], a[28:31], v[192:195], v[92:95]\n" \
  "s_add_u32 s16, s53, 0x0\n" \
  "buffer_load_dwordx4 a[164:167], v18, s[36:39], s16 offen sc1\n" \
  "v_mfma_f32_16x16x32_bf16 v[100:103], a[32:35], v[192:195], v[100:103]\n" \
  "ds_read_b128 a[24:27], v23 offset:20480\n" \
  "s_waitcnt vmcnt(25)\n" \
  "v_mfma_f32_16x16x32_bf16 v[96:99], a[32:35], v[188:191], v[96:99]\n" \
  "v_accvgpr_read_b32 v164, a56\n" \
  "v_accvgpr_read_b32 v168, a57\n" \
  "v_mfma_f32_16x16x32_bf16 v[44:47], a[32:35], v[184:187], v[44:47]\n" \
  "v_accvgpr_read_b32 v172, a58\n" \
  "v_accvgpr_read_b32 v176, a59\n" \
  "v_mfma_f32_16x16x32_bf16 v[40:43], a[32:35], v[180:183], v[40:43]\n" \
  "buffer_load_dwordx4 a[168:171], v19, s[36:39], s16 offen sc1\n" \
  "v_accvgpr_read_b32 v165, a60\n" \
  "v_mfma_f32_16x16x32_bf16 v[48:51], a[36:39], v[180:183], v[48:51]\n" \
  "v_accvgpr_read_b32 v169, a61\n" \
  "v_accvgpr_read_b32 v173, a62\n" \
  "v_mfma_f32_16x16x32_bf16 v[52:55], a[36:39], v[184:187], v[52:55]\n" \
  "v_accvgpr_read_b32 v177, a63\n" \
  "v_accvgpr_read_b32 v166, a64\n" \
  "v_mfma_f32_16x16x32_bf16 v[104:107], a[36:39], v[188:191], v[104:107]\n" \
  "v_accvgpr_read_b32 v170, a65\n" \
  "v_accvgpr_read_b32 v174, a66\n" \
  "v_mfma_f32_16x16x32_bf16 v[108:111], a[36:39], v[192:195], v[108:111]\n" \
  "buffer_load_dwordx4 a[172:175], v20, s[36:39], s16 offen sc1\n" \
  "v_accvgpr_read_b32 v178, a67\n" \
  "s_waitcnt lgkmcnt(7)\n" \
  "v_mfma_f32_16x16x32_bf16 v[116:119], a[40:43], v[192:195], v[116:119]\n" \
  "v_accvgpr_read_b32 v167, a68\n" \
  "v_accvgpr_read_b32 v171, a69\n" \
  "v_mfma_f32_16x16x32_bf16 v[112:115], a[40:43], v[188:191], v[112:115]\n" \
  "v_accvgpr_read_b32 v175, a70\n" \
  "v_accvgpr_read_b32 v179, a71\n" \
  "v_mfma_f32_16x16x32_bf16 v[60:63], a[40:43], v[184:187], v[60:63]\n" \
  "buffer_load_dwordx4 a[176:179], v21, s[36:39], s16 offen sc1\n" \
  "v_max3_u32 v2, v164, v165, v166\n" \
  "v_mfma_f32_16x16x32_bf16 v[56:59], a[40:43], v[180:183], v[56:59]\n" \
  "v_max_u32 v2, v2, v167\n" \
  "v_cmp_ne_u32 vcc, -1, v2\n" \
  "v_mfma_f32_16x16x32_bf16 v[64:67], a[44:47], v[180:183], v[64:67]\n" \
  "s_cmp_eq_u64 vcc, -1\n" \
  "s_cbranch_scc0 LS10_slow%=\n" \
  "LS10_ok%=:\n" \
  "v_mfma_f32_16x16x32_bf16 v[68:71], a[44:47], v[184:187], v[68:71]\n" \
  "v_mfma_f32_16x16x32_bf16 v[120:123], a[44:47], v[188:191], v[120:123]\n" \
  "s_mov_b32 s14, 0x938000\n" \
  "buffer_load_dwordx4 a[56:59], v14, s[36:39], s14 offen sc1\n" \
  "v_mfma_f32_16x16x32_bf16 v[124:127], a[44:47], v[192:195], v[124:127]\n" \
  "v_mfma_f32_16x16x32_bf16 v[132:135], a[48:51], v[192:195], v[132:135]\n" \
  "v_mfma_f32_16x16x32_bf16 v[128:131], a[48:51], v[188:191], v[128:131]\n" \
  "v_mfma_f32_16x16x32_bf16 v[76:79], a[48:51], v[184:187], v[76:79]\n" \
  "buffer_load_dwordx4 a[60:63], v15, s[36:39], s14 offen sc1\n" \
  "v_mfma_f32_16x16x32_bf16 v[72:75], a[48:51], v[180:183], v[72:75]\n" \
  "v_mfma_f32_16x16x32_bf16 v[80:83], a[52:55], v[180:183], v[80:83]\n" \
  "v_mfma_f32_16x16x32_bf16 v[84:87], a[52:55], v[184:187], v[84:87]\n" \
  "v_mfma_f32_16x16x32_bf16 v[136:139], a[52:55], v[188:191], v[136:139]\n" \
  "buffer_load_dwordx4 a[64:67], v16, s[36:39], s14 offen sc1\n" \
  "v_mfma_f32_16x16x32_bf16 v[140:143], a[52:55], v[192:195], v[140:143]\n" \
  "v_mfma_f32_16x16x32_bf16 v[156:159], v[192:195], v[192:195], v[156:159]\n" \
  "v_mfma_f32_16x16x32_bf16 v[152:155], v[188:191], v[188:191], v[152:155]\n" \
  "v_mfma_f32_16x16x32_bf16 v[148:151], v[184:187], v[184:187], v[148:151]\n" \
  "buffer_load_dwordx4 a[68:71], v17, s[36:39], s14 offen sc1\n" \
  "v_mfma_f32_16x16x32_bf16 v[144:147], v[180:183], v[180:183], v[144:147]\n" \
  "s_waitcnt lgkmcnt(4)\n" \
  "v_mfma_f32_16x16x32_bf16 v[32:35], a[0:3], v[164:167], v[32:35]\n" \
  "ds_read_b128 a[28:31], v23 offset:21504\n" \
  "ds_read_b128 a[32:35], v23 offset:22528\n" \
  "v_mfma_f32_16x16x32_bf16 v[36:39], a[0:3], v[168:171], v[36:39]\n" \
  "ds_read_b128 a[36:39], v23 offset:23552\n" \
  "ds_read_b128 a[40:43], v23 offset:24576\n" \
  "v_mfma_f32_16x16x32_bf16 v[88:91], a[0:3], v[172:175], v[88:91]\n" \
  "s_add_u32 s16, s53, 0x8000\n" \
  "buffer_load_dwordx4 a[188:191], v18, s[36:39], s16 offen sc1\n" \
  "v_mfma_f32_16x16x32_bf16 v[92:95], a[0:3], v[176:179], v[92:95]\n" \
  "ds_read_b128 a[44:47], v23 offset:25600\n" \
  "ds_read_b128 a[48:51], v23 offset:26624\n" \
  "v_mfma_f32_16x16x32_bf16 v[100:103], a[4:7], v[176:179], v[100:103]\n" \
  "ds_read_b128 a[52:55], v23 offset:27648\n" \
  "s_waitcnt vmcnt(29)\n" \
  "v_mfma_f32_16x16x32_bf16 v[96:99], a[4:7], v[172:175], v[96:99]\n" \
  "v_accvgpr_read_b32 v180, a72\n" \
  "v_accvgpr_read_b32 v184, a73\n" \
  "v_mfma_f32_16x16x32_bf16 v[44:47], a[4:7], v[168:171], v[44:47]\n" \
  "buffer_load_dwordx4 a[192:195], v19, s[36:39], s16 offen sc1\n" \
  "v_accvgpr_mov_b32 a180, a74\n" \
  "v_mfma_f32_16x16x32_bf16 v[40:43], a[4:7], v[164:167], v[40:43]\n" \
  "v_accvgpr_mov_b32 a184, a75\n" \
  "v_accvgpr_read_b32 v181, a76\n" \
  "v_mfma_f32_16x16x32_bf16 v[48:51], a[8:11], v[164:167], v[48:51]\n" \
  "v_accvgpr_read_b32 v185, a77\n" \
  "v_accvgpr_mov_b32 a181, a78\n" \
  "v_mfma_f32_16x16x32_bf16 v[52:55], a[8:11], v[168:171], v[52:55]\n" \
  "v_accvgpr_mov_b32 a185, a79\n" \
  "v_accvgpr_read_b32 v182, a80\n" \
  "v_mfma_f32_16x16x32_bf16 v[104:107], a[8:11], v[172:175], v[104:107]\n" \
  "buffer_load_dwordx4 a[196:199], v20, s[36:39], s16 offen sc1\n" \
  "v_accvgpr_read_b32 v186, a81\n" \
  "v_mfma_f32_16x16x32_bf16 v[108:111], a[8:11], v[176:179], v[108:111]\n" \
  "v_accvgpr_mov_b32 a182, a82\n" \
  "v_accvgpr_mov_b32 a186, a83\n" \
  "s_waitcnt lgkmcnt(7)\n" \
  "v_mfma_f32_16x16x32_bf16 v[116:119], a[12:15], v[176:179], v[116:119]\n" \
  "v_accvgpr_read_b32 v183, a84\n" \
  "v_accvgpr_read_b32 v187, a85\n" \
  "v_mfma_f32_16x16x32_bf16 v[112:115], a[12:15], v[172:175], v[112:115]\n" \
  "v_accvgpr_mov_b32 a183, a86\n" \
  "v_accvgpr_mov_b32 a187, a87\n" \
  "v_mfma_f32_16x16x32_bf16 v[60:63], a[12:15], v[168:171], v[60:63]\n" \
  "buffer_load_dwordx4 a[200:203], v21, s[36:39], s16 offen sc1\n" \
  "v_max3_u32 v2, v180, v181, v182\n" \
  "v_mfma_f32_16x16x32_bf16 v[56:59], a[12:15], v[164:167], v[56:59]\n" \
  "v_max_u32 v2, v2, v183\n" \
  "v_cmp_ne_u32 vcc, -1, v2\n" \
  "v_mfma_f32_16x16x32_bf16 v[64:67], a[16:19], v[164:167], v[64:67]\n" \
  "s_cmp_eq_u64 vcc, -1\n" \
  "s_cbranch_scc0 LS11_slow%=\n" \
  "LS11_ok%=:\n" \
  "v_mfma_f32_16x16x32_bf16 v[68:71], a[16:19], v[168:171], v[68:71]\n" \
  "v_mfma_f32_16x16x32_bf16 v[120:123], a[16:19], v[172:175], v[120:123]\n" \
  "s_add_u32 s16, s53, 0x10000\n" \
  "buffer_load_dwordx4 a[204:207], v18, s[36:39], s16 offen sc1\n" \
  "v_mfma_f32_16x16x32_bf16 v[124:127], a[16:19], v[176:179], v[124:127]\n" \
  "v_mfma_f32_16x16x32_bf16 v[132:135], a[20:23], v[176:179], v[132:135]\n" \
  "v_mfma_f32_16x16x32_bf16 v[128:131], a[20:23], v[172:175], v[128:131]\n" \
  "v_mfma_f32_16x16x32_bf16 v[76:79], a[20:23], v[168:171], v[76:79]\n" \
  "buffer_load_dwordx4 a[208:211], v19, s[36:39], s16 offen sc1\n" \
  "v_mfma_f32_16x16x32_bf16 v[72:75], a[20:23], v[164:167], v[72:75]\n" \
  "v_mfma_f32_16x16x32_bf16 v[80:83], a[24:27], v[164:167], v[80:83]\n" \
  "v_mfma_f32_16x16x32_bf16 v[84:87], a[24:27], v[168:171], v[84:87]\n" \
  "v_mfma_f32_16x16x32_bf16 v[136:139], a[24:27], v[172:175], v[136:139]\n" \
  "buffer_load_dwordx4 a[212:215], v20, s[36:39], s16 offen sc1\n" \
  "v_mfma_f32_16x16x32_bf16 v[140:143], a[24:27], v[176:179], v[140:143]\n" \
  "v_mfma_f32_16x16x32_bf16 v[156:159], v[176:179], v[176:179], v[156:159]\n" \
  "v_mfma_f32_16x16x32_bf16 v[152:155], v[172:175], v[172:175], v[152:155]\n" \
  "v_mfma_f32_16x16x32_bf16 v[148:151], v[168:171], v[168:171], v[148:151]\n" \
  "buffer_load_dwordx4 a[216:219], v21, s[36:39], s16 offen sc1\n" \
  "v_mfma_f32_16x16x32_bf16 v[144:147], v[164:167], v[164:167], v[144:147]\n" \
  "s_waitcnt lgkmcnt(4)\n" \
  "v_mfma_f32_16x16x32_bf16 v[32:35], a[28:31], v[180:183], v[32:35]\n" \
  "ds_read_b128 a[0:3], v23 offset:28672\n" \
  "ds_read_b128 a[4:7], v23 offset:29696\n" \
  "v_mfma_f32_16x16x32_bf16 v[36:39], a[28:31], v[184:187], v[36:39]\n" \
  "ds_read_b128 a[8:11], v23 offset:30720\n" \
  "ds_read_b128 a[12:15], v23 offset:31744\n" \
  "v_mfma_f32_16x16x32_bf16 v[44:47], a[32:35], v[184:187], v[44:47]\n" \
  "s_add_u32 s16, s53, 0x18000\n" \
  "buffer_load_dwordx4 a[236:239], v18, s[36:39], s16 offen sc1\n" \
  "v_mfma_f32_16x16x32_bf16 v[40:43], a[32:35], v[180:183], v[40:43]\n" \
  "ds_read_b128 a[16:19], v23 offset:32768\n" \
  "ds_read_b128 a[20:23], v23 offset:33792\n" \
  "v_mfma_f32_16x16x32_bf16 v[48:51], a[36:39], v[180:183], v[48:51]\n" \
  "ds_read_b128 a[24:27], v23 offset:34816\n" \
  "s_waitcnt vmcnt(31)\n" \
  "v_mfma_f32_16x16x32_bf16 v[52:55], a[36:39], v[184:187], v[52:55]\n" \
  "v_accvgpr_read_b32 v164, a88\n" \
  "v_accvgpr_read_b32 v168, a89\n" \
  "s_waitcnt lgkmcnt(7)\n" \
  "v_mfma_f32_16x16x32_bf16 v[60:63], a[40:43], v[184:187], v[60:63]\n" \
  "buffer_load_dwordx4 a[240:243], v19, s[36:39], s16 offen sc1\n" \
  "v_accvgpr_mov_b32 a220, a90\n" \
  "v_mfma_f32_16x16x32_bf16 v[56:59], a[40:43], v[180:183], v[56:59]\n" \
  "v_accvgpr_mov_b32 a232, a91\n" \
  "v_accvgpr_read_b32 v165, a92\n" \
  "v_mfma_f32_16x16x32_bf16 v[64:67], a[44:47], v[180:183], v[64:67]\n" \
  "v_accvgpr_read_b32 v169, a93\n" \
  "v_accvgpr_mov_b32 a221, a94\n" \
  "v_mfma_f32_16x16x32_bf16 v[68:71], a[44:47], v[184:187], v[68:71]\n" \
  "buffer_load_dwordx4 a[244:247], v20, s[36:39], s16 offen sc1\n" \
  "v_accvgpr_mov_b32 a233, a95\n" \
  "v_mfma_f32_16x16x32_bf16 v[76:79], a[48:51], v[184:187], v[76:79]\n" \
  "v_accvgpr_read_b32 v166, a96\n" \
  "v_accvgpr_read_b32 v170, a97\n" \
  "v_mfma_f32_16x16x32_bf16 v[72:75], a[48:51], v[180:183], v[72:75]\n" \
  "v_accvgpr_mov_b32 a222, a98\n" \
  "v_accvgpr_mov_b32 a234, a99\n" \
  "v_mfma_f32_16x16x32_bf16 v[80:83], a[52:55], v[180:183], v[80:83]\n" \
  "v_accvgpr_read_b32 v167, a100\n" \
  "v_accvgpr_read_b32 v171, a101\n" \
  "v_mfma_f32_16x16x32_bf16 v[84:87], a[52:55], v[184:187], v[84:87]\n" \
  "buffer_load_dwordx4 a[248:251], v21, s[36:39], s16 offen sc1\n" \
  "v_accvgpr_mov_b32 a223, a102\n" \
  "v_mfma_f32_16x16x32_bf16 v[156:159], a[184:187], a[184:187], v[156:159]\n" \
  "v_accvgpr_mov_b32 a235, a103\n" \
  "v_max3_u32 v2, v164, v165, v166\n" \
  "v_mfma_f32_16x16x32_bf16 v[152:155], a[180:183], a[180:183], v[152:155]\n" \
  "v_max_u32 v2, v2, v167\n" \
  "v_cmp_ne_u32 vcc, -1, v2\n" \
  "v_mfma_f32_16x16x32_bf16 v[148:151], v[184:187], v[184:187], v[148:151]\n" \
  "s_cmp_eq_u64 vcc, -1\n" \
  "s_cbranch_scc0 LS12_slow%=\n" \
  "LS12_ok%=:\n" \
  "v_mfma_f32_16x16x32_bf16 v[144:147], v[180:183], v[180:183], v[144:147]\n" \
  "buffer_load_dwordx4 a[252:255], v24, s[36:39], s15 offen offset:256 sc1\n" \
  "s_waitcnt lgkmcnt(4)\n" \
  "v_mfma_f32_16x16x32_bf16 v[32:35], a[0:3], v[164:167], v[32:35]\n" \
  "ds_read_b128 a[28:31], v23 offset:35840\n" \
  "ds_read_b128 a[32:35], v23 offset:36864\n" \
  "v_mfma_f32_16x16x32_bf16 v[36:39], a[0:3], v[168:171], v[36:39]\n" \
  "ds_read_b128 a[36:39], v23 offset:37888\n" \
  "ds_read_b128 a[40:43], v23 offset:38912\n" \
  "v_mfma_f32_16x16x32_bf16 v[44:47], a[4:7], v[168:171], v[44:47]\n" \
  "ds_read_b128 a[44:47], v23 offset:39936\n" \
  "ds_read_b128 a[48:51], v23 offset:40960\n" \
  "v_mfma_f32_16x16x32_bf16 v[40:43], a[4:7], v[164:167], v[40:43]\n" \
  "buffer_load_dwordx4 a[72:75], v24, s[36:39], s15 offen offset:512 sc1\n" \
  "ds_read_b128 a[52:55], v23 offset:41984\n" \
  "v_mfma_f32_16x16x32_bf16 v[48:51], a[8:11], v[164:167], v[48:51]\n" \
  "s_waitcnt vmcnt(30)\n" \
  "v_accvgpr_read_b32 v180, a104\n" \
  "v_mfma_f32_16x16x32_bf16 v[52:55], a[8:11], v[168:171], v[52:55]\n" \
  "v_accvgpr_read_b32 v184, a105\n" \
  "v_accvgpr_mov_b32 a224, a106\n" \
  "s_waitcnt lgkmcnt(7)\n" \
  "v_mfma_f32_16x16x32_bf16 v[60:63], a[12:15], v[168:171], v[60:63]\n" \
  "v_accvgpr_mov_b32 a228, a107\n" \
  "v_accvgpr_read_b32 v181, a108\n" \
  "v_mfma_f32_16x16x32_bf16 v[56:59], a[12:15], v[164:167], v[56:59]\n" \
  "buffer_load_dwordx4 a[76:79], v24, s[36:39], s15 offen offset:768 sc1\n" \
  "v_accvgpr_read_b32 v185, a109\n" \
  "v_mfma_f32_16x16x32_bf16 v[64:67], a[16:19], v[164:167], v[64:67]\n" \
  "v_accvgpr_mov_b32 a225, a110\n" \
  "v_accvgpr_mov_b32 a229, a111\n" \
  "v_mfma_f32_16x16x32_bf16 v[68:71], a[16:19], v[168:171], v[68:71]\n" \
  "v_accvgpr_read_b32 v182, a112\n" \
  "v_accvgpr_read_b32 v186, a113\n" \
  "v_mfma_f32_16x16x32_bf16 v[76:79], a[20:23], v[168:171], v[76:79]\n" \
  "v_accvgpr_mov_b32 a226, a114\n" \
  "v_accvgpr_mov_b32 a230, a115\n" \
  "v_mfma_f32_16x16x32_bf16 v[72:75], a[20:23], v[164:167], v[72:75]\n" \
  "s_mov_b32 s15, 0x1802000\n" \
  "buffer_load_dwordx4 a[80:83], v24, s[36:39], s15 offen sc1\n" \
  "v_mfma_f32_16x16x32_bf16 v[80:83], a[24:27], v[164:167], v[80:83]\n" \
  "v_accvgpr_read_b32 v183, a116\n" \
  "v_accvgpr_read_b32 v187, a117\n" \
  "v_mfma_f32_16x16x32_bf16 v[84:87], a[24:27], v[168:171], v[84:87]\n" \
  "v_accvgpr_mov_b32 a227, a118\n" \
  "v_accvgpr_mov_b32 a231, a119\n" \
  "v_mfma_f32_16x16x32_bf16 v[156:159], a[232:235], a[232:235], v[156:159]\n" \
  "v_max3_u32 v2, v180, v181, v182\n" \
  "v_max_u32 v2, v2, v183\n" \
  "v_mfma_f32_16x16x32_bf16 v[152:155], a[220:223], a[220:223], v[152:155]\n" \
  "buffer_load_dwordx4 a[84:87], v24, s[36:39], s15 offen offset:256 sc1\n" \
  "v_cmp_ne_u32 vcc, -1, v2\n" \
  "v_mfma_f32_16x16x32_bf16 v[148:151], v[168:171], v[168:171], v[148:151]\n" \
  "s_cmp_eq_u64 vcc, -1\n" \
  "s_cbranch_scc0 LS13_slow%=\n" \
  "LS13_ok%=:\n" \
  "v_mfma_f32_16x16x32_bf16 v[144:147], v[164:167], v[164:167], v[144:147]\n" \
  "s_waitcnt lgkmcnt(4)\n" \
  "v_mfma_f32_16x16x32_bf16 v[32:35], a[28:31], v[180:183], v[32:35]\n" \
  "ds_read_b128 a[0:3], v23 offset:43008\n" \
  "ds_read_b128 a[4:7], v23 offset:44032\n" \
  "v_mfma_f32_16x16x32_bf16 v[36:39], a[28:31], v[184:187], v[36:39]\n" \
  "buffer_load_dwordx4 a[96:99], v24, s[36:39], s15 offen offset:512 sc1\n" \
  "ds_read_b128 a[8:11], v23 offset:45056\n" \
  "v_mfma_f32_16x16x32_bf16 v[44:47], a[32:35], v[184:187], v[44:47]\n" \
  "ds_read_b128 a[12:15], v23 offset:46080\n" \
  "ds_read_b128 a[16:19], v23 offset:47104\n" \
  "v_mfma_f32_16x16x32_bf16 v[40:43], a[32:35], v[180:183], v[40:43]\n" \
  "ds_read_b128 a[20:23], v23 offset:48128\n" \
  "ds_read_b128 a[24:27], v23 offset:49152\n" \
  "v_mfma_f32_16x16x32_bf16 v[48:51], a[36:39], v[180:183], v[48:51]\n" \
  "s_waitcnt vmcnt(26)\n" \
  "v_accvgpr_read_b32 v164, a120\n" \
  "v_mfma_f32_16x16x32_bf16 v[52:55], a[36:39], v[184:187], v[52:55]\n" \
  "buffer_load_dwordx4 a[100:103], v24, s[36:39], s15 offen offset:768 sc1\n" \
  "v_accvgpr_read_b32 v168, a121\n" \
  "s_waitcnt lgkmcnt(7)\n" \
  "v_mfma_f32_16x16x32_bf16 v[60:63], a[40:43], v[184:187], v[60:63]\n" \
  "v_accvgpr_mov_b32 a88, a122\n" \
  "v_accvgpr_mov_b32 a92, a123\n" \
  "v_mfma_f32_16x16x32_bf16 v[56:59], a[40:43], v[180:183], v[56:59]\n" \
  "v_accvgpr_read_b32 v165, a124\n" \
  "v_accvgpr_read_b32 v169, a125\n" \
  "v_mfma_f32_16x16x32_bf16 v[64:67], a[44:47], v[180:183], v[64:67]\n" \
  "s_mov_b32 s15, 0x1803000\n" \
  "buffer_load_dwordx4 a[104:107], v24, s[36:39], s15 offen sc1\n" \
  "v_mfma_f32_16x16x32_bf16 v[68:71], a[44:47], v[184:187], v[68:71]\n" \
  "v_accvgpr_mov_b32 a89, a126\n" \
  "v_accvgpr_mov_b32 a93, a127\n" \
  "v_mfma_f32_16x16x32_bf16 v[76:79], a[48:51], v[184:187], v[76:79]\n" \
  "v_accvgpr_read_b32 v166, a128\n" \
  "v_accvgpr_read_b32 v170, a129\n" \
  "v_mfma_f32_16x16x32_bf16 v[72:75], a[48:51], v[180:183], v[72:75]\n" \
  "v_accvgpr_mov_b32 a90, a130\n" \
  "v_accvgpr_mov_b32 a94, a131\n" \
  "v_mfma_f32_16x16x32_bf16 v[80:83], a[52:55], v[180:183], v[80:83]\n" \
  "buffer_load_dwordx4 a[108:111], v24, s[36:39], s15 offen offset:256 sc1\n" \
  "v_accvgpr_read_b32 v167, a132\n" \
  "v_mfma_f32_16x16x32_bf16 v[84:87], a[52:55], v[184:187], v[84:87]\n" \
  "v_accvgpr_read_b32 v171, a133\n" \
  "v_accvgpr_mov_b32 a91, a134\n" \
  "v_mfma_f32_16x16x32_bf16 v[156:159], a[228:231], a[228:231], v[156:159]\n" \
  "v_accvgpr_mov_b32 a95, a135\n" \
  "v_max3_u32 v2, v164, v165, v166\n" \
  "v_mfma_f32_16x16x32_bf16 v[152:155], a[224:227], a[224:227], v[152:155]\n" \
  "v_max_u32 v2, v2, v167\n" \
  "v_cmp_ne_u32 vcc, -1, v2\n" \
  "v_mfma_f32_16x16x32_bf16 v[148:151], v[184:187], v[184:187], v[148:151]\n" \
  "buffer_load_dwordx4 a[112:115], v24, s[36:39], s15 offen offset:512 sc1\n" \
  "s_cmp_eq_u64 vcc, -1\n" \
  "v_mfma_f32_16x16x32_bf16 v[144:147], v[180:183], v[180:183], v[144:147]\n" \
  "s_cbranch_scc0 LS14_slow%=\n" \
  "LS14_ok%=:\n" \
  "s_waitcnt lgkmcnt(4)\n" \
  "v_mfma_f32_16x16x32_bf16 v[32:35], a[0:3], v[164:167], v[32:35]\n" \
  "ds_read_b128 a[28:31], v23 offset:50176\n" \
  "ds_read_b128 a[32:35], v23 offset:51200\n" \
  "v_mfma_f32_16x16x32_bf16 v[36:39], a[0:3], v[168:171], v[36:39]\n" \
  "ds_read_b128 a[36:39], v23 offset:52224\n" \
  "ds_read_b128 a[40:43], v23 offset:53248\n" \
  "v_mfma_f32_16x16x32_bf16 v[44:47], a[4:7], v[168:171], v[44:47]\n" \
  "buffer_load_dwordx4 a[124:127], v24, s[36:39], s15 offen offset:768 sc1\n" \
  "ds_read_b128 a[44:47], v23 offset:54272\n" \
  "v_mfma_f32_16x16x32_bf16 v[40:43], a[4:7], v[164:167], v[40:43]\n" \
  "ds_read_b128 a[48:51], v23 offset:55296\n" \
  "ds_read_b128 a[52:55], v23 offset:56320\n" \
  "v_mfma_f32_16x16x32_bf16 v[48:51], a[8:11], v[164:167], v[48:51]\n" \
  "s_waitcnt vmcnt(23)\n" \
  "v_accvgpr_read_b32 v180, a56\n" \
  "v_mfma_f32_16x16x32_bf16 v[52:55], a[8:11], v[168:171], v[52:55]\n" \
  "v_accvgpr_read_b32 v184, a57\n" \
  "v_accvgpr_mov_b32 a116, a58\n" \
  "s_waitcnt lgkmcnt(7)\n" \
  "v_mfma_f32_16x16x32_bf16 v[60:63], a[12:15], v[168:171], v[60:63]\n" \
  "s_mov_b32 s15, 0x1804000\n" \
  "buffer_load_dwordx4 a[128:131], v24, s[36:39], s15 offen sc1\n" \
  "v_mfma_f32_16x16x32_bf16 v[56:59], a[12:15], v[164:167], v[56:59]\n" \
  "v_accvgpr_mov_b32 a120, a59\n" \
  "v_accvgpr_read_b32 v181, a60\n" \
  "v_mfma_f32_16x16x32_bf16 v[64:67], a[16:19], v[164:167], v[64:67]\n" \
  "v_accvgpr_read_b32 v185, a61\n" \
  "v_accvgpr_mov_b32 a117, a62\n" \
  "v_mfma_f32_16x16x32_bf16 v[68:71], a[16:19], v[168:171], v[68:71]\n" \
  "v_accvgpr_mov_b32 a121, a63\n" \
  "v_accvgpr_read_b32 v182, a64\n" \
  "v_mfma_f32_16x16x32_bf16 v[76:79], a[20:23], v[168:171], v[76:79]\n" \
  "buffer_load_dwordx4 a[132:135], v24, s[36:39], s15 offen offset:256 sc1\n" \
  "v_accvgpr_read_b32 v186, a65\n" \
  "v_mfma_f32_16x16x32_bf16 v[72:75], a[20:23], v[164:167], v[72:75]\n" \
  "v_accvgpr_mov_b32 a118, a66\n" \
  "v_accvgpr_mov_b32 a122, a67\n" \
  "v_mfma_f32_16x16x32_bf16 v[80:83], a[24:27], v[164:167], v[80:83]\n" \
  "v_accvgpr_read_b32 v183, a68\n" \
  "v_accvgpr_read_b32 v187, a69\n" \
  "v_mfma_f32_16x16x32_bf16 v[84:87], a[24:27], v[168:171], v[84:87]\n" \
  "v_accvgpr_mov_b32 a119, a70\n" \
  "v_accvgpr_mov_b32 a123, a71\n" \
  "v_mfma_f32_16x16x32_bf16 v[156:159], a[92:95], a[92:95], v[156:159]\n" \
  "buffer_load_dwordx4 v[228:231], v24, s[36:39], s15 offen offset:512 sc1\n" \
  "v_max3_u32 v2, v180, v181, v182\n" \
  "v_mfma_f32_16x16x32_bf16 v[152:155], a[88:91], a[88:91], v[152:155]\n" \
  "v_max_u32 v2, v2, v183\n" \
  "v_cmp_ne_u32 vcc, -1, v2\n" \
  "v_mfma_f32_16x16x32_bf16 v[148:151], v[168:171], v[168:171], v[148:151]\n" \
  "s_cmp_eq_u64 vcc, -1\n" \
  "s_cbranch_scc0 LS15_slow%=\n" \
  "LS15_ok%=:\n" \
  "v_mfma_f32_16x16x32_bf16 v[144:147], v[164:167], v[164:167], v[144:147]\n" \
  "s_waitcnt lgkmcnt(4)\n" \
  "v_mfma_f32_16x16x32_bf16 v[32:35], a[28:31], v[180:183], v[32:35]\n" \
  "buffer_load_dwordx4 a[56:59], v24, s[36:39], s15 offen offset:768 sc1\n" \
  "ds_read_b128 a[0:3], v23 offset:21504\n" \
  "v_mfma_f32_16x16x32_bf16 v[36:39], a[28:31], v[184:187], v[36:39]\n" \
  "ds_read_b128 a[4:7], v23 offset:22528\n" \
  "ds_read_b128 a[8:11], v23 offset:23552\n" \
  "v_mfma_f32_16x16x32_bf16 v[44:47], a[32:35], v[184:187], v[44:47]\n" \
  "ds_read_b128 a[12:15], v23 offset:24576\n" \
  "ds_read_b128 a[16:19], v23 offset:25600\n" \
  "v_mfma_f32_16x16x32_bf16 v[40:43], a[32:35], v[180:183], v[40:43]\n" \
  "ds_read_b128 a[20:23], v23 offset:26624\n" \
  "ds_read_b128 a[24:27], v23 offset:27648\n" \
  "v_mfma_f32_16x16x32_bf16 v[48:51], a[36:39], v[180:183], v[48:51]\n" \
  "s_mov_b32 s15, 0x1805000\n" \
  "buffer_load_dwordx4 a[60:63], v24, s[36:39], s15 offen sc1\n" \
  "v_mfma_f32_16x16x32_bf16 v[52:55], a[36:39], v[184:187], v[52:55]\n" \
  "s_waitcnt lgkmcnt(7)\n" \
  "v_mfma_f32_16x16x32_bf16 v[60:63], a[40:43], v[184:187], v[60:63]\n" \
  "v_mfma_f32_16x16x32_bf16 v[56:59], a[40:43], v[180:183], v[56:59]\n" \
  "v_mfma_f32_16x16x32_bf16 v[64:67], a[44:47], v[180:183], v[64:67]\n" \
  "buffer_load_dwordx4 a[64:67], v24, s[36:39], s15 offen offset:256 sc1\n" \
  "v_mfma_f32_16x16x32_bf16 v[68:71], a[44:47], v[184:187], v[68:71]\n" \
  "v_mfma_f32_16x16x32_bf16 v[76:79], a[48:51], v[184:187], v[76:79]\n" \
  "v_mfma_f32_16x16x32_bf16 v[72:75], a[48:51], v[180:183], v[72:75]\n" \
  "v_mfma_f32_16x16x32_bf16 v[80:83], a[52:55], v[180:183], v[80:83]\n" \
  "buffer_load_dwordx4 a[68:71], v24, s[36:39], s15 offen offset:512 sc1\n" \
  "v_mfma_f32_16x16x32_bf16 v[84:87], a[52:55], v[184:187], v[84:87]\n" \
  "v_mfma_f32_16x16x32_bf16 v[156:159], a[120:123], a[120:123], v[156:159]\n" \
  "v_mfma_f32_16x16x32_bf16 v[152:155], a[116:119], a[116:119], v[152:155]\n" \
  "v_mfma_f32_16x16x32_bf16 v[148:151], v[184:187], v[184:187], v[148:151]\n" \
  "buffer_load_dwordx4 v[232:235], v24, s[36:39], s15 offen offset:768 sc1\n" \
  "v_mfma_f32_16x16x32_bf16 v[144:147], v[180:183], v[180:183], v[144:147]\n" \
  "s_waitcnt lgkmcnt(4)\n" \
  "v_mfma_f32_16x16x32_bf16 v[88:91], a[0:3], a[180:183], v[88:91]\n" \
  "ds_read_b128 a[28:31], v23 offset:28672\n" \
  "ds_read_b128 a[32:35], v23 offset:29696\n" \
  "ds_read_b128 a[36:39], v23 offset:30720\n" \
  "ds_read_b128 a[40:43], v23 offset:31744\n" \
  "v_mfma_f32_16x16x32_bf16 v[92:95], a[0:3], a[184:187], v[92:95]\n" \
  "s_mov_b32 s15, 0x1806000\n" \
  "buffer_load_dwordx4 v[176:179], v24, s[36:39], s15 offen sc1\n" \
  "ds_read_b128 a[44:47], v23 offset:32768\n" \
  "ds_read_b128 a[48:51], v23 offset:33792\n" \
  "v_mfma_f32_16x16x32_bf16 v[100:103], a[4:7], a[184:187], v[100:103]\n" \
  "ds_read_b128 a[52:55], v23 offset:34816\n" \
  "v_cndmask_b32 v236, v144, v145, s[54:55]\n" \
  "v_cndmask_b32 v240, v152, v153, s[54:55]\n" \
  "v_permlane32_swap_b32 v32, v36\n" \
  "v_mfma_f32_16x16x32_bf16 v[96:99], a[4:7], a[180:183], v[96:99]\n" \
  "v_cndmask_b32 v238, v148, v149, s[54:55]\n" \
  "v_cndmask_b32 v242, v156, v157, s[54:55]\n" \
  "v_permlane32_swap_b32 v33, v37\n" \
  "v_cndmask_b32 v237, v146, v147, s[54:55]\n" \
  "v_mfma_f32_16x16x32_bf16 v[104:107], a[8:11], a[180:183], v[104:107]\n" \
  "buffer_load_dwordx4 v[180:183], v24, s[36:39], s15 offen offset:256 sc1\n" \
  "v_cndmask_b32 v241, v154, v155, s[54:55]\n" \
  "v_permlane32_swap_b32 v34, v38\n" \
  "v_cndmask_b32 v239, v150, v151, s[54:55]\n" \
  "v_mfma_f32_16x16x32_bf16 v[108:111], a[8:11], a[184:187], v[108:111]\n" \
  "v_cndmask_b32 v243, v158, v159, s[54:55]\n" \
  "v_permlane32_swap_b32 v35, v39\n" \
  "v_cndmask_b32 v236, v236, v237, s[56:57]\n" \
  "v_cndmask_b32 v240, v240, v241, s[56:57]\n" \
  "s_waitcnt lgkmcnt(7)\n" \
  "v_mfma_f32_16x16x32_bf16 v[116:119], a[12:15], a[184:187], v[116:119]\n" \
  "buffer_load_dwordx4 v[184:187], v24, s[36:39], s15 offen offset:512 sc1\n" \
  "v_permlane32_swap_b32 v40, v44\n" \
  "v_cndmask_b32 v238, v238, v239, s[56:57]\n" \
  "v_cndmask_b32 v242, v242, v243, s[56:57]\n" \
  "v_mfma_f32_16x16x32_bf16 v[112:115], a[12:15], a[180:183], v[112:115]\n" \
  "v_permlane32_swap_b32 v41, v45\n" \
  "v_cndmask_b32 v236, 0, v236, s[58:59]\n" \
  "v_cndmask_b32 v240, 0, v240, s[58:59]\n" \
  "v_permlane32_swap_b32 v42, v46\n" \
  "v_mfma_f32_16x16x32_bf16 v[120:123], a[16:19], a[180:183], v[120:123]\n" \
  "v_cndmask_b32 v238, 0, v238, s[58:59]\n" \
  "v_cndmask_b32 v242, 0, v242, s[58:59]\n" \
  "v_permlane32_swap_b32 v43, v47\n" \
  "v_mov_b32 v237, v236\n" \
  "v_mfma_f32_16x16x32_bf16 v[124:127], a[16:19], a[184:187], v[124:127]\n" \
  "buffer_load_dwordx4 v[188:191], v24, s[36:39], s15 offen offset:768 sc1\n" \
  "v_mov_b32 v241, v240\n" \
  "v_permlane32_swap_b32 v48, v52\n" \
  "v_mov_b32 v239, v238\n" \
  "v_mfma_f32_16x16x32_bf16 v[132:135], a[20:23], a[184:187], v[132:135]\n" \
  "v_mov_b32 v243, v242\n" \
  "v_permlane32_swap_b32 v49, v53\n" \
  "v_permlane16_swap_b32 v236, v237\n" \
  "v_permlane16_swap_b32 v240, v241\n" \
  "v_mfma_f32_16x16x32_bf16 v[128:131], a[20:23], a[180:183], v[128:131]\n" \
  "v_permlane32_swap_b32 v50, v54\n" \
  "v_permlane16_swap_b32 v238, v239\n" \
  "v_permlane16_swap_b32 v242, v243\n" \
  "v_permlane32_swap_b32 v51, v55\n" \
  "v_mfma_f32_16x16x32_bf16 v[136:139], a[24:27], a[180:183], v[136:139]\n" \
  "s_mov_b32 s15, 0x1807000\n" \
  "buffer_load_dwordx4 v[192:195], v24, s[36:39], s15 offen sc1\n" \
  "v_add_f32 v236, v236, v237\n" \
  "v_add_f32 v240, v240, v241\n" \
  "v_mfma_f32_16x16x32_bf16 v[140:143], a[24:27], a[184:187], v[140:143]\n" \
  "v_permlane32_swap_b32 v56, v60\n" \
  "v_add_f32 v238, v238, v239\n" \
  "v_add_f32 v242, v242, v243\n" \
  "v_permlane32_swap_b32 v57, v61\n" \
  "v_permlane32_swap_b32 v236, v238\n" \
  "v_permlane32_swap_b32 v240, v242\n" \
  "v_permlane32_swap_b32 v58, v62\n" \
  "v_add_f32 v236, v236, v238\n" \
  "s_waitcnt lgkmcnt(4)\n" \
  "v_mfma_f32_16x16x32_bf16 v[88:91], a[28:31], a[220:223], v[88:91]\n" \
  "buffer_load_dwordx4 a[180:183], v24, s[36:39], s15 offen offset:256 sc1\n" \
  "ds_read_b128 a[0:3], v23 offset:35840\n" \
  "ds_read_b128 a[4:7], v23 offset:36864\n" \
  "ds_read_b128 a[8:11], v23 offset:37888\n" \
  "v_mfma_f32_16x16x32_bf16 v[92:95], a[28:31], a[232:235], v[92:95]\n" \
  "ds_read_b128 a[12:15], v23 offset:38912\n" \
  "ds_read_b128 a[16:19], v23 offset:39936\n" \
  "ds_read_b128 a[20:23], v23 offset:40960\n" \
  "ds_read_b128 a[24:27], v23 offset:41984\n" \
  "v_mfma_f32_16x16x32_bf16 v[100:103], a[32:35], a[232:235], v[100:103]\n" \
  "buffer_load_dwordx4 a[184:187], v24, s[36:39], s15 offen offset:512 sc1\n" \
  "v_add_f32 v240, v240, v242\n" \
  "v_permlane32_swap_b32 v59, v63\n" \
  "v_mul_f32 v236, 0x3b000000, v236\n" \
  "v_mfma_f32_16x16x32_bf16 v[96:99], a[32:35], a[220:223], v[96:99]\n" \
  "v_mul_f32 v240, 0x3b000000, v240\n" \
  "v_add_f32 v236, 0x3727c5ac, v236\n" \
  "v_add_f32 v240, 0x3727c5ac, v240\n" \
  "v_rsq_f32 v238, v236\n" \
  "v_mfma_f32_16x16x32_bf16 v[104:107], a[36:39], a[220:223], v[104:107]\n" \
  "v_rsq_f32 v242, v240\n" \
  "v_mul_f32 v15, v236, v238\n" \
  "v_mul_f32 v17, v240, v242\n" \
  "v_mfma_f32_16x16x32_bf16 v[108:111], a[36:39], a[232:235], v[108:111]\n" \
  "v_mul_f32 v14, 0xbfb8aa3b, v238\n" \
  "v_mul_f32 v16, 0xbfb8aa3b, v242\n" \
  "v_mul_f32 v236, v32, v14\n" \
  "v_mul_f32 v237, v33, v14\n" \
  "s_waitcnt lgkmcnt(7)\n" \
  "v_mfma_f32_16x16x32_bf16 v[116:119], a[40:43], a[232:235], v[116:119]\n" \
  "v_mul_f32 v238, v34, v14\n" \
  "v_mul_f32 v239, v35, v14\n" \
  "v_mul_f32 v240, v40, v14\n" \
  "v_mul_f32 v241, v41, v14\n" \
  "v_mfma_f32_16x16x32_bf16 v[112:115], a[40:43], a[220:223], v[112:115]\n" \
  "v_mul_f32 v242, v42, v14\n" \
  "v_mul_f32 v243, v43, v14\n" \
  "v_mul_f32 v244, v48, v14\n" \
  "v_mul_f32 v245, v49, v14\n" \
  "v_mfma_f32_16x16x32_bf16 v[120:123], a[44:47], a[220:223], v[120:123]\n" \
  "v_mul_f32 v246, v50, v14\n" \
  "v_mul_f32 v247, v51, v14\n" \
  "v_mul_f32 v248, v56, v14\n" \
  "v_mul_f32 v249, v57, v14\n" \
  "v_mfma_f32_16x16x32_bf16 v[124:127], a[44:47], a[232:235], v[124:127]\n" \
  "v_mul_f32 v250, v58, v14\n" \
  "v_mul_f32 v251, v59, v14\n" \
  "v_exp_f32 v236, v236\n" \
  "v_mfma_f32_16x16x32_bf16 v[132:135], a[48:51], a[232:235], v[132:135]\n" \
  "v_exp_f32 v237, v237\n" \
  "v_exp_f32 v238, v238\n" \
  "v_mfma_f32_16x16x32_bf16 v[128:131], a[48:51], a[220:223], v[128:131]\n" \
  "v_exp_f32 v239, v239\n" \
  "v_exp_f32 v240, v240\n" \
  "v_mfma_f32_16x16x32_bf16 v[136:139], a[52:55], a[220:223], v[136:139]\n" \
  "v_exp_f32 v241, v241\n" \
  "v_exp_f32 v242, v242\n" \
  "v_mfma_f32_16x16x32_bf16 v[140:143], a[52:55], a[232:235], v[140:143]\n" \
  "v_exp_f32 v243, v243\n" \
  "s_waitcnt lgkmcnt(4)\n" \
  "v_mfma_f32_16x16x32_bf16 v[88:91], a[0:3], a[224:227], v[88:91]\n" \
  "buffer_load_dwordx4 a[220:223], v24, s[36:39], s15 offen offset:768 sc1\n" \
  "ds_read_b128 a[28:31], v23 offset:43008\n" \
  "ds_read_b128 a[32:35], v23 offset:44032\n" \
  "ds_read_b128 a[36:39], v23 offset:45056\n" \
  "v_mfma_f32_16x16x32_bf16 v[92:95], a[0:3], a[228:231], v[92:95]\n" \
  "s_mov_b32 s15, 0x1808000\n" \
  "buffer_load_dwordx4 a[232:235], v24, s[36:39], s15 offen sc1\n" \
  "ds_read_b128 a[40:43], v23 offset:46080\n" \
  "ds_read_b128 a[44:47], v23 offset:47104\n" \
  "v_mfma_f32_16x16x32_bf16 v[100:103], a[4:7], a[228:231], v[100:103]\n" \
  "buffer_load_dwordx4 v[144:147], v24, s[36:39], s15 offen offset:256 sc1\n" \
  "ds_read_b128 a[48:51], v23 offset:48128\n" \
  "ds_read_b128 a[52:55], v23 offset:49152\n" \
  "v_exp_f32 v244, v244\n" \
  "v_mfma_f32_16x16x32_bf16 v[96:99], a[4:7], a[224:227], v[96:99]\n" \
  "v_exp_f32 v245, v245\n" \
  "v_exp_f32 v246, v246\n" \
  "v_mfma_f32_16x16x32_bf16 v[104:107], a[8:11], a[224:227], v[104:107]\n" \
  "buffer_load_dwordx4 v[148:151], v24, s[36:39], s15 offen offset:512 sc1\n" \
  "v_exp_f32 v247, v247\n" \
  "v_exp_f32 v248, v248\n" \
  "v_mfma_f32_16x16x32_bf16 v[108:111], a[8:11], a[228:231], v[108:111]\n" \
  "v_exp_f32 v249, v249\n" \
  "v_exp_f32 v250, v250\n" \
  "s_waitcnt lgkmcnt(7)\n" \
  "v_mfma_f32_16x16x32_bf16 v[116:119], a[12:15], a[228:231], v[116:119]\n" \
  "v_exp_f32 v251, v251\n" \
  "v_fma_f32 v236, v236, v15, v15\n" \
  "v_fma_f32 v237, v237, v15, v15\n" \
  "v_mfma_f32_16x16x32_bf16 v[112:115], a[12:15], a[224:227], v[112:115]\n" \
  "buffer_load_dwordx4 v[152:155], v24, s[36:39], s15 offen offset:768 sc1\n" \
  "v_fma_f32 v238, v238, v15, v15\n" \
  "v_fma_f32 v239, v239, v15, v15\n" \
  "v_fma_f32 v240, v240, v15, v15\n" \
  "v_mfma_f32_16x16x32_bf16 v[120:123], a[16:19], a[224:227], v[120:123]\n" \
  "v_fma_f32 v241, v241, v15, v15\n" \
  "v_fma_f32 v242, v242, v15, v15\n" \
  "v_fma_f32 v243, v243, v15, v15\n" \
  "v_fma_f32 v244, v244, v15, v15\n" \
  "v_mfma_f32_16x16x32_bf16 v[124:127], a[16:19], a[228:231], v[124:127]\n" \
  "s_mov_b32 s15, 0x1809000\n" \
  "buffer_load_dwordx4 v[156:159], v24, s[36:39], s15 offen sc1\n" \
  "v_fma_f32 v245, v245, v15, v15\n" \
  "v_fma_f32 v246, v246, v15, v15\n" \
  "v_mfma_f32_16x16x32_bf16 v[132:135], a[20:23], a[228:231], v[132:135]\n" \
  "v_fma_f32 v247, v247, v15, v15\n" \
  "v_fma_f32 v248, v248, v15, v15\n" \
  "v_fma_f32 v249, v249, v15, v15\n" \
  "v_fma_f32 v250, v250, v15, v15\n" \
  "v_mfma_f32_16x16x32_bf16 v[128:131], a[20:23], a[224:227], v[128:131]\n" \
  "v_fma_f32 v251, v251, v15, v15\n" \
  "v_rcp_f32 v236, v236\n" \
  "v_rcp_f32 v237, v237\n" \
  "v_mfma_f32_16x16x32_bf16 v[136:139], a[24:27], a[224:227], v[136:139]\n" \
  "v_rcp_f32 v238, v238\n" \
  "v_rcp_f32 v239, v239\n" \
  "v_mfma_f32_16x16x32_bf16 v[140:143], a[24:27], a[228:231], v[140:143]\n" \
  "v_rcp_f32 v240, v240\n" \
  "v_rcp_f32 v241, v241\n" \
  "v_rcp_f32 v242, v242\n" \
  "s_waitcnt lgkmcnt(4)\n" \
  "v_mfma_f32_16x16x32_bf16 v[88:91], a[28:31], a[88:91], v[88:91]\n" \
  "buffer_load_dwordx4 a[224:227], v24, s[36:39], s15 offen offset:256 sc1\n" \
  "ds_read_b128 a[0:3], v23 offset:50176\n" \
  "ds_read_b128 a[4:7], v23 offset:51200\n" \
  "ds_read_b128 a[8:11], v23 offset:52224\n" \
  "v_mfma_f32_16x16x32_bf16 v[92:95], a[28:31], a[92:95], v[92:95]\n" \
  "buffer_load_dwordx4 a[228:231], v24, s[36:39], s15 offen offset:512 sc1\n" \
  "ds_read_b128 a[12:15], v23 offset:53248\n" \
  "ds_read_b128 a[16:19], v23 offset:54272\n" \
  "ds_read_b128 a[20:23], v23 offset:55296\n" \
  "v_mfma_f32_16x16x32_bf16 v[100:103], a[32:35], a[92:95], v[100:103]\n" \
  "ds_read_b128 a[24:27], v23 offset:56320\n" \
  "v_rcp_f32 v243, v243\n" \
  "v_rcp_f32 v244, v244\n" \
  "v_mfma_f32_16x16x32_bf16 v[96:99], a[32:35], a[88:91], v[96:99]\n" \
  "v_rcp_f32 v245, v245\n" \
  "v_rcp_f32 v246, v246\n" \
  "v_mfma_f32_16x16x32_bf16 v[104:107], a[36:39], a[88:91], v[104:107]\n" \
  "v_rcp_f32 v247, v247\n" \
  "v_rcp_f32 v248, v248\n" \
  "v_mfma_f32_16x16x32_bf16 v[108:111], a[36:39], a[92:95], v[108:111]\n" \
  "v_rcp_f32 v249, v249\n" \
  "v_rcp_f32 v250, v250\n" \
  "s_waitcnt lgkmcnt(7)\n" \
  "v_mfma_f32_16x16x32_bf16 v[116:119], a[40:43], a[92:95], v[116:119]\n" \
  "v_rcp_f32 v251, v251\n" \
  "v_mul_f32 v236, v236, v36\n" \
  "v_mul_f32 v237, v237, v37\n" \
  "v_mfma_f32_16x16x32_bf16 v[112:115], a[40:43], a[88:91], v[112:115]\n" \
  "v_mul_f32 v238, v238, v38\n" \
  "v_mul_f32 v239, v239, v39\n" \
  "v_mul_f32 v240, v240, v44\n" \
  "v_mul_f32 v241, v241, v45\n" \
  "v_mfma_f32_16x16x32_bf16 v[120:123], a[44:47], a[88:91], v[120:123]\n" \
  "v_mul_f32 v242, v242, v46\n" \
  "v_mul_f32 v243, v243, v47\n" \
  "v_mul_f32 v244, v244, v52\n" \
  "v_mul_f32 v245, v245, v53\n" \
  "v_mfma_f32_16x16x32_bf16 v[124:127], a[44:47], a[92:95], v[124:127]\n" \
  "v_mul_f32 v246, v246, v54\n" \
  "v_mul_f32 v247, v247, v55\n" \
  "v_mul_f32 v248, v248, v60\n" \
  "v_mul_f32 v249, v249, v61\n" \
  "v_mfma_f32_16x16x32_bf16 v[132:135], a[48:51], a[92:95], v[132:135]\n" \
  "v_mul_f32 v250, v250, v62\n" \
  "v_mul_f32 v251, v251, v63\n" \
  "v_cvt_pk_bf16_f32 v252, v236, v237\n" \
  "v_cvt_pk_bf16_f32 v253, v238, v239\n" \
  "v_mfma_f32_16x16x32_bf16 v[128:131], a[48:51], a[88:91], v[128:131]\n" \
  "v_cvt_pk_bf16_f32 v254, v240, v241\n" \
  "v_cvt_pk_bf16_f32 v255, v242, v243\n" \
  "v_cvt_pk_bf16_f32 v164, v244, v245\n" \
  "v_cvt_pk_bf16_f32 v165, v246, v247\n" \
  "v_mfma_f32_16x16x32_bf16 v[136:139], a[52:55], a[88:91], v[136:139]\n" \
  "v_cvt_pk_bf16_f32 v166, v248, v249\n" \
  "v_cvt_pk_bf16_f32 v167, v250, v251\n" \
  "v_permlane16_swap_b32 v252, v254\n" \
  "v_permlane16_swap_b32 v253, v255\n" \
  "v_mfma_f32_16x16x32_bf16 v[140:143], a[52:55], a[92:95], v[140:143]\n" \
  "v_permlane16_swap_b32 v164, v166\n" \
  "v_permlane16_swap_b32 v165, v167\n" \
  "s_waitcnt lgkmcnt(4)\n" \
  "v_mfma_f32_16x16x32_bf16 v[88:91], a[0:3], a[116:119], v[88:91]\n" \
  "v_permlane32_swap_b32 v64, v68\n" \
  "v_permlane32_swap_b32 v65, v69\n" \
  "v_permlane32_swap_b32 v66, v70\n" \
  "v_permlane32_swap_b32 v67, v71\n" \
  "v_mfma_f32_16x16x32_bf16 v[92:95], a[0:3], a[120:123], v[92:95]\n" \
  "v_permlane32_swap_b32 v72, v76\n" \
  "v_permlane32_swap_b32 v73, v77\n" \
  "v_permlane32_swap_b32 v74, v78\n" \
  "v_permlane32_swap_b32 v75, v79\n" \
  "v_mfma_f32_16x16x32_bf16 v[100:103], a[4:7], a[120:123], v[100:103]\n" \
  "v_permlane32_swap_b32 v80, v84\n" \
  "v_permlane32_swap_b32 v81, v85\n" \
  "v_permlane32_swap_b32 v82, v86\n" \
  "v_permlane32_swap_b32 v83, v87\n" \
  "v_mfma_f32_16x16x32_bf16 v[96:99], a[4:7], a[116:119], v[96:99]\n" \
  "v_mul_f32 v236, v64, v14\n" \
  "v_mul_f32 v237, v65, v14\n" \
  "v_mul_f32 v238, v66, v14\n" \
  "v_mul_f32 v239, v67, v14\n" \
  "v_mfma_f32_16x16x32_bf16 v[104:107], a[8:11], a[116:119], v[104:107]\n" \
  "v_mul_f32 v240, v72, v14\n" \
  "v_mul_f32 v241, v73, v14\n" \
  "v_mul_f32 v242, v74, v14\n" \
  "v_mul_f32 v243, v75, v14\n" \
  "v_mfma_f32_16x16x32_bf16 v[108:111], a[8:11], a[120:123], v[108:111]\n" \
  "v_mul_f32 v244, v80, v14\n" \
  "v_mul_f32 v245, v81, v14\n" \
  "v_mul_f32 v246, v82, v14\n" \
  "v_mul_f32 v247, v83, v14\n" \
  "s_waitcnt lgkmcnt(0)\n" \
  "v_mfma_f32_16x16x32_bf16 v[116:119], a[12:15], a[120:123], v[116:119]\n" \
  "v_exp_f32 v236, v236\n" \
  "v_exp_f32 v237, v237\n" \
  "v_mfma_f32_16x16x32_bf16 v[112:115], a[12:15], a[116:119], v[112:115]\n" \
  "v_exp_f32 v238, v238\n" \
  "v_exp_f32 v239, v239\n" \
  "v_mfma_f32_16x16x32_bf16 v[120:123], a[16:19], a[116:119], v[120:123]\n" \
  "v_exp_f32 v240, v240\n" \
  "v_exp_f32 v241, v241\n" \
  "v_mfma_f32_16x16x32_bf16 v[124:127], a[16:19], a[120:123], v[124:127]\n" \
  "v_exp_f32 v242, v242\n" \
  "v_exp_f32 v243, v243\n" \
  "v_mfma_f32_16x16x32_bf16 v[132:135], a[20:23], a[120:123], v[132:135]\n" \
  "v_exp_f32 v244, v244\n" \
  "v_exp_f32 v245, v245\n" \
  "v_mfma_f32_16x16x32_bf16 v[128:131], a[20:23], a[116:119], v[128:131]\n" \
  "v_exp_f32 v246, v246\n" \
  "v_exp_f32 v247, v247\n" \
  "v_mfma_f32_16x16x32_bf16 v[136:139], a[24:27], a[116:119], v[136:139]\n" \
  "v_fma_f32 v236, v236, v15, v15\n" \
  "v_fma_f32 v237, v237, v15, v15\n" \
  "v_fma_f32 v238, v238, v15, v15\n" \
  "v_fma_f32 v239, v239, v15, v15\n" \
  "v_mfma_f32_16x16x32_bf16 v[140:143], a[24:27], a[120:123], v[140:143]\n" \
  "v_fma_f32 v240, v240, v15, v15\n" \
  "v_fma_f32 v241, v241, v15, v15\n" \
  "v_fma_f32 v242, v242, v15, v15\n" \
  "v_fma_f32 v243, v243, v15, v15\n" \
  "v_fma_f32 v244, v244, v15, v15\n" \
  "v_fma_f32 v245, v245, v15, v15\n" \
  "v_fma_f32 v246, v246, v15, v15\n" \
  "v_fma_f32 v247, v247, v15, v15\n" \
  "v_rcp_f32 v236, v236\n" \
  "v_rcp_f32 v237, v237\n" \
  "v_rcp_f32 v238, v238\n" \
  "v_rcp_f32 v239, v239\n" \
  "v_rcp_f32 v240, v240\n" \
  "v_rcp_f32 v241, v241\n" \
  "v_rcp_f32 v242, v242\n" \
  "v_rcp_f32 v243, v243\n" \
  "v_rcp_f32 v244, v244\n" \
  "v_rcp_f32 v245, v245\n" \
  "v_rcp_f32 v246, v246\n" \
  "v_rcp_f32 v247, v247\n" \
  "v_mul_f32 v236, v236, v68\n" \
  "v_mul_f32 v237, v237, v69\n" \
  "v_mul_f32 v238, v238, v70\n" \
  "v_mul_f32 v239, v239, v71\n" \
  "v_mul_f32 v240, v240, v76\n" \
  "v_mul_f32 v241, v241, v77\n" \
  "v_mul_f32 v242, v242, v78\n" \
  "v_mul_f32 v243, v243, v79\n" \
  "v_mul_f32 v244, v244, v84\n" \
  "v_mul_f32 v245, v245, v85\n" \
  "v_mul_f32 v246, v246, v86\n" \
  "v_mul_f32 v247, v247, v87\n" \
  "v_cvt_pk_bf16_f32 v168, v236, v237\n" \
  "v_cvt_pk_bf16_f32 v169, v238, v239\n" \
  "v_cvt_pk_bf16_f32 v170, v240, v241\n" \
  "v_cvt_pk_bf16_f32 v171, v242, v243\n" \
  "v_cvt_pk_bf16_f32 v172, v244, v245\n" \
  "v_cvt_pk_bf16_f32 v173, v246, v247\n" \
  "v_permlane16_swap_b32 v168, v170\n" \
  "v_permlane16_swap_b32 v169, v171\n" \
  "v_permlane16_swap_b32 v172, v174\n" \
  "v_permlane16_swap_b32 v173, v175\n" \
  "s_waitcnt vmcnt(0)\n" \
  "s_add_u32 s62, s50, 0x0\n" \
  "buffer_store_dwordx4 v[252:255], v4, s[36:39], s62 offen sc1\n" \
  "buffer_store_dwordx4 v[164:167], v4, s[36:39], s62 offen offset:512 sc1\n" \
  "buffer_store_dwordx4 v[168:171], v4, s[36:39], s62 offen offset:1024 sc1\n" \
  "buffer_store_dwordx4 v[172:175], v25, s[36:39], s62 offen offset:1536 sc1\n" \
  "v_permlane32_swap_b32 v88, v92\n" \
  "v_permlane32_swap_b32 v89, v93\n" \
  "v_permlane32_swap_b32 v90, v94\n" \
  "v_permlane32_swap_b32 v91, v95\n" \
  "v_permlane32_swap_b32 v96, v100\n" \
  "v_permlane32_swap_b32 v97, v101\n" \
  "v_permlane32_swap_b32 v98, v102\n" \
  "v_permlane32_swap_b32 v99, v103\n" \
  "v_permlane32_swap_b32 v104, v108\n" \
  "v_permlane32_swap_b32 v105, v109\n" \
  "v_permlane32_swap_b32 v106, v110\n" \
  "v_permlane32_swap_b32 v107, v111\n" \
  "v_permlane32_swap_b32 v112, v116\n" \
  "v_permlane32_swap_b32 v113, v117\n" \
  "v_permlane32_swap_b32 v114, v118\n" \
  "v_permlane32_swap_b32 v115, v119\n" \
  "v_pk_mul_f32 v[236:237], v[88:89], v[16:17] op_sel:[0,0] op_sel_hi:[1,0]\n" \
  "v_pk_mul_f32 v[238:239], v[90:91], v[16:17] op_sel:[0,0] op_sel_hi:[1,0]\n" \
  "v_pk_mul_f32 v[240:241], v[96:97], v[16:17] op_sel:[0,0] op_sel_hi:[1,0]\n" \
  "v_pk_mul_f32 v[242:243], v[98:99], v[16:17] op_sel:[0,0] op_sel_hi:[1,0]\n" \
  "v_pk_mul_f32 v[244:245], v[104:105], v[16:17] op_sel:[0,0] op_sel_hi:[1,0]\n" \
  "v_pk_mul_f32 v[246:247], v[106:107], v[16:17] op_sel:[0,0] op_sel_hi:[1,0]\n" \
  "v_pk_mul_f32 v[248:249], v[112:113], v[16:17] op_sel:[0,0] op_sel_hi:[1,0]\n" \
  "v_pk_mul_f32 v[250:251], v[114:115], v[16:17] op_sel:[0,0] op_sel_hi:[1,0]\n" \
  "v_exp_f32 v236, v236\n" \
  "v_exp_f32 v237, v237\n" \
  "v_exp_f32 v238, v238\n" \
  "v_exp_f32 v239, v239\n" \
  "v_exp_f32 v240, v240\n" \
  "v_exp_f32 v241, v241\n" \
  "v_exp_f32 v242, v242\n" \
  "v_exp_f32 v243, v243\n" \
  "v_exp_f32 v244, v244\n" \
  "v_exp_f32 v245, v245\n" \
  "v_exp_f32 v246, v246\n" \
  "v_exp_f32 v247, v247\n" \
  "v_exp_f32 v248, v248\n" \
  "v_exp_f32 v249, v249\n" \
  "v_exp_f32 v250, v250\n" \
  "v_exp_f32 v251, v251\n" \
  "v_pk_fma_f32 v[236:237], v[236:237], v[16:17], v[16:17] op_sel:[0,1,1] op_sel_hi:[1,1,1]\n" \
  "v_pk_fma_f32 v[238:239], v[238:239], v[16:17], v[16:17] op_sel:[0,1,1] op_sel_hi:[1,1,1]\n" \
  "v_pk_fma_f32 v[240:241], v[240:241], v[16:17], v[16:17] op_sel:[0,1,1] op_sel_hi:[1,1,1]\n" \
  "v_pk_fma_f32 v[242:243], v[242:243], v[16:17], v[16:17] op_sel:[0,1,1] op_sel_hi:[1,1,1]\n" \
  "v_pk_fma_f32 v[244:245], v[244:245], v[16:17], v[16:17] op_sel:[0,1,1] op_sel_hi:[1,1,1]\n" \
  "v_pk_fma_f32 v[246:247], v[246:247], v[16:17], v[16:17] op_sel:[0,1,1] op_sel_hi:[1,1,1]\n" \
  "v_pk_fma_f32 v[248:249], v[248:249], v[16:17], v[16:17] op_sel:[0,1,1] op_sel_hi:[1,1,1]\n" \
  "v_pk_fma_f32 v[250:251], v[250:251], v[16:17], v[16:17] op_sel:[0,1,1] op_sel_hi:[1,1,1]\n" \
  "v_rcp_f32 v236, v236\n" \
  "v_rcp_f32 v237, v237\n" \
  "v_rcp_f32 v238, v238\n" \
  "v_rcp_f32 v239, v239\n" \
  "v_rcp_f32 v240, v240\n" \
  "v_rcp_f32 v241, v241\n" \
  "v_rcp_f32 v242, v242\n" \
  "v_rcp_f32 v243, v243\n" \
  "v_rcp_f32 v244, v244\n" \
  "v_rcp_f32 v245, v245\n" \
  "v_rcp_f32 v246, v246\n" \
  "v_rcp_f32 v247, v247\n" \
  "v_rcp_f32 v248, v248\n" \
  "v_rcp_f32 v249, v249\n" \
  "v_rcp_f32 v250, v250\n" \
  "v_rcp_f32 v251, v251\n" \
  "v_pk_mul_f32 v[236:237], v[236:237], v[92:93]\n" \
  "v_pk_mul_f32 v[238:239], v[238:239], v[94:95]\n" \
  "v_pk_mul_f32 v[240:241], v[240:241], v[100:101]\n" \
  "v_pk_mul_f32 v[242:243], v[242:243], v[102:103]\n" \
  "v_pk_mul_f32 v[244:245], v[244:245], v[108:109]\n" \
  "v_pk_mul_f32 v[246:247], v[246:247], v[110:111]\n" \
  "v_pk_mul_f32 v[248:249], v[248:249], v[116:117]\n" \
  "v_pk_mul_f32 v[250:251], v[250:251], v[118:119]\n" \
  "v_cvt_pk_bf16_f32 v252, v236, v237\n" \
  "v_cvt_pk_bf16_f32 v253, v238, v239\n" \
  "v_cvt_pk_bf16_f32 v254, v240, v241\n" \
  "v_cvt_pk_bf16_f32 v255, v242, v243\n" \
  "v_cvt_pk_bf16_f32 v164, v244, v245\n" \
  "v_cvt_pk_bf16_f32 v165, v246, v247\n" \
  "v_cvt_pk_bf16_f32 v166, v248, v249\n" \
  "v_cvt_pk_bf16_f32 v167, v250, v251\n" \
  "v_permlane16_swap_b32 v252, v254\n" \
  "v_permlane16_swap_b32 v253, v255\n" \
  "v_permlane16_swap_b32 v164, v166\n" \
  "v_permlane16_swap_b32 v165, v167\n" \
  "v_permlane32_swap_b32 v120, v124\n" \
  "v_permlane32_swap_b32 v121, v125\n" \
  "v_permlane32_swap_b32 v122, v126\n" \
  "v_permlane32_swap_b32 v123, v127\n" \
  "v_permlane32_swap_b32 v128, v132\n" \
  "v_permlane32_swap_b32 v129, v133\n" \
  "v_permlane32_swap_b32 v130, v134\n" \
  "v_permlane32_swap_b32 v131, v135\n" \
  "v_permlane32_swap_b32 v136, v140\n" \
  "v_permlane32_swap_b32 v137, v141\n" \
  "v_permlane32_swap_b32 v138, v142\n" \
  "v_permlane32_swap_b32 v139, v143\n" \
  "v_pk_mul_f32 v[236:237], v[120:121], v[16:17] op_sel:[0,0] op_sel_hi:[1,0]\n" \
  "v_pk_mul_f32 v[238:239], v[122:123], v[16:17] op_sel:[0,0] op_sel_hi:[1,0]\n" \
  "v_pk_mul_f32 v[240:241], v[128:129], v[16:17] op_sel:[0,0] op_sel_hi:[1,0]\n" \
  "v_pk_mul_f32 v[242:243], v[130:131], v[16:17] op_sel:[0,0] op_sel_hi:[1,0]\n" \
  "v_pk_mul_f32 v[244:245], v[136:137], v[16:17] op_sel:[0,0] op_sel_hi:[1,0]\n" \
  "v_pk_mul_f32 v[246:247], v[138:139], v[16:17] op_sel:[0,0] op_sel_hi:[1,0]\n" \
  "v_exp_f32 v236, v236\n" \
  "v_exp_f32 v237, v237\n" \
  "v_exp_f32 v238, v238\n" \
  "v_exp_f32 v239, v239\n" \
  "v_exp_f32 v240, v240\n" \
  "v_exp_f32 v241, v241\n" \
  "v_exp_f32 v242, v242\n" \
  "v_exp_f32 v243, v243\n" \
  "v_exp_f32 v244, v244\n" \
  "v_exp_f32 v245, v245\n" \
  "v_exp_f32 v246, v246\n" \
  "v_exp_f32 v247, v247\n" \
  "v_pk_fma_f32 v[236:237], v[236:237], v[16:17], v[16:17] op_sel:[0,1,1] op_sel_hi:[1,1,1]\n" \
  "v_pk_fma_f32 v[238:239], v[238:239], v[16:17], v[16:17] op_sel:[0,1,1] op_sel_hi:[1,1,1]\n" \
  "v_pk_fma_f32 v[240:241], v[240:241], v[16:17], v[16:17] op_sel:[0,1,1] op_sel_hi:[1,1,1]\n" \
  "v_pk_fma_f32 v[242:243], v[242:243], v[16:17], v[16:17] op_sel:[0,1,1] op_sel_hi:[1,1,1]\n" \
  "v_pk_fma_f32 v[244:245], v[244:245], v[16:17], v[16:17] op_sel:[0,1,1] op_sel_hi:[1,1,1]\n" \
  "v_pk_fma_f32 v[246:247], v[246:247], v[16:17], v[16:17] op_sel:[0,1,1] op_sel_hi:[1,1,1]\n" \
  "s_waitcnt vmcnt(0)\n" \
  "buffer_store_dwordx2 v[10:11], v12, s[36:39], s51 offen sc1\n" \
  "v_rcp_f32 v236, v236\n" \
  "v_rcp_f32 v237, v237\n" \
  "v_rcp_f32 v238, v238\n" \
  "v_rcp_f32 v239, v239\n" \
  "v_rcp_f32 v240, v240\n" \
  "v_rcp_f32 v241, v241\n" \
  "v_rcp_f32 v242, v242\n" \
  "v_rcp_f32 v243, v243\n" \
  "v_rcp_f32 v244, v244\n" \
  "v_rcp_f32 v245, v245\n" \
  "v_rcp_f32 v246, v246\n" \
  "v_rcp_f32 v247, v247\n" \
  "v_pk_mul_f32 v[236:237], v[236:237], v[124:125]\n" \
  "v_pk_mul_f32 v[238:239], v[238:239], v[126:127]\n" \
  "v_pk_mul_f32 v[240:241], v[240:241], v[132:133]\n" \
  "v_pk_mul_f32 v[242:243], v[242:243], v[134:135]\n" \
  "v_pk_mul_f32 v[244:245], v[244:245], v[140:141]\n" \
  "v_pk_mul_f32 v[246:247], v[246:247], v[142:143]\n" \
  "v_cvt_pk_bf16_f32 v168, v236, v237\n" \
  "v_cvt_pk_bf16_f32 v169, v238, v239\n" \
  "v_cvt_pk_bf16_f32 v170, v240, v241\n" \
  "v_cvt_pk_bf16_f32 v171, v242, v243\n" \
  "v_cvt_pk_bf16_f32 v172, v244, v245\n" \
  "v_cvt_pk_bf16_f32 v173, v246, v247\n" \
  "v_permlane16_swap_b32 v168, v170\n" \
  "v_permlane16_swap_b32 v169, v171\n" \
  "v_permlane16_swap_b32 v172, v174\n" \
  "v_permlane16_swap_b32 v173, v175\n" \
  "s_add_u32 s62, s50, 0x1c000\n" \
  "buffer_store_dwordx4 v[252:255], v4, s[36:39], s62 offen sc1\n" \
  "v_mov_b32 v32, v163\n" \
  "v_add_f32 v33, 0x3c000000, v163\n" \
  "v_add_f32 v34, 0x3c800000, v163\n" \
  "v_add_f32 v35, 0x3cc00000, v163\n" \
  "v_mul_f32 v36, v162, v32\n" \
  "v_mul_f32 v37, v162, v33\n" \
  "buffer_store_dwordx4 v[164:167], v4, s[36:39], s62 offen offset:512 sc1\n" \
  "v_mul_f32 v38, v162, v34\n" \
  "v_mul_f32 v39, v162, v35\n" \
  "v_accvgpr_read_b32 v40, a164\n" \
  "v_accvgpr_read_b32 v41, a168\n" \
  "v_accvgpr_read_b32 v42, a172\n" \
  "v_accvgpr_read_b32 v43, a176\n" \
  "buffer_store_dwordx4 v[168:171], v4, s[36:39], s62 offen offset:1024 sc1\n" \
  "v_accvgpr_read_b32 v44, a165\n" \
  "v_accvgpr_read_b32 v45, a169\n" \
  "v_accvgpr_read_b32 v46, a173\n" \
  "v_accvgpr_read_b32 v47, a177\n" \
  "v_accvgpr_read_b32 v48, a166\n" \
  "v_accvgpr_read_b32 v49, a170\n" \
  "buffer_store_dwordx4 v[172:175], v25, s[36:39], s62 offen offset:1536 sc1\n" \
  "v_accvgpr_read_b32 v50, a174\n" \
  "v_accvgpr_read_b32 v51, a178\n" \
  "v_accvgpr_read_b32 v52, a167\n" \
  "v_accvgpr_read_b32 v53, a171\n" \
  "v_accvgpr_read_b32 v54, a175\n" \
  "v_accvgpr_read_b32 v55, a179\n" \
  "v_add_u32 v14, 0xe000, v22\n" \
  "v_add_u32 v15, 0x1c000, v22\n" \
  "v_add_u32 v16, 0x2a000, v22\n" \
  "v_mfma_f32_16x16x32_bf16 a[88:91], v[196:199], v[40:43], 0\n" \
  "v_fmamk_f32 v237, v32, 0x41000000, v36\n" \
  "v_fmamk_f32 v239, v32, 0x42200000, v36\n" \
  "v_fmamk_f32 v241, v32, 0x42900000, v36\n" \
  "v_fmamk_f32 v243, v32, 0x42d00000, v36\n" \
  "v_fmamk_f32 v245, v33, 0x41000000, v37\n" \
  "v_fmamk_f32 v247, v33, 0x42200000, v37\n" \
  "v_mfma_f32_16x16x32_bf16 a[92:95], v[200:203], v[40:43], 0\n" \
  "buffer_load_dwordx4 a[48:51], v24, s[36:39], s15 offen offset:768 sc1\n" \
  "v_fmamk_f32 v249, v33, 0x42900000, v37\n" \
  "v_fmamk_f32 v251, v33, 0x42d00000, v37\n" \
  "v_cos_f32 v236, v237\n" \
  "v_cos_f32 v238, v239\n" \
  "v_mfma_f32_16x16x32_bf16 a[116:119], v[204:207], v[40:43], 0\n" \
  "v_cos_f32 v240, v241\n" \
  "v_cos_f32 v242, v243\n" \
  "v_cos_f32 v244, v245\n" \
  "v_mfma_f32_16x16x32_bf16 a[120:123], v[208:211], v[40:43], 0\n" \
  "s_mov_b32 s15, 0x180a000\n" \
  "buffer_load_dwordx4 a[52:55], v24, s[36:39], s15 offen sc1\n" \
  "v_cos_f32 v246, v247\n" \
  "v_cos_f32 v248, v249\n" \
  "v_mfma_f32_16x16x32_bf16 a[0:3], v[196:199], v[44:47], 0\n" \
  "v_cos_f32 v250, v251\n" \
  "v_sin_f32 v237, -v237\n" \
  "v_sin_f32 v239, -v239\n" \
  "v_mfma_f32_16x16x32_bf16 a[4:7], v[200:203], v[44:47], 0\n" \
  "buffer_load_dwordx4 a[164:167], v24, s[36:39], s15 offen offset:256 sc1\n" \
  "v_sin_f32 v241, -v241\n" \
  "v_sin_f32 v243, -v243\n" \
  "v_sin_f32 v245, -v245\n" \
  "v_mfma_f32_16x16x32_bf16 a[8:11], v[204:207], v[44:47], 0\n" \
  "v_sin_f32 v247, -v247\n" \
  "v_sin_f32 v249, -v249\n" \
  "v_sin_f32 v251, -v251\n" \
  "v_mfma_f32_16x16x32_bf16 a[12:15], v[208:211], v[44:47], 0\n" \
  "buffer_load_dwordx4 a[168:171], v24, s[36:39], s15 offen offset:512 sc1\n" \
  "v_cvt_pk_bf16_f32 v56, v236, v237\n" \
  "v_cvt_pk_bf16_f32 v57, v238, v239\n" \
  "v_cvt_pk_bf16_f32 v58, v240, v241\n" \
  "v_cvt_pk_bf16_f32 v59, v242, v243\n" \
  "v_cvt_pk_bf16_f32 v60, v244, v245\n" \
  "v_mfma_f32_16x16x32_bf16 a[16:19], v[196:199], v[48:51], 0\n" \
  "v_cvt_pk_bf16_f32 v61, v246, v247\n" \
  "v_cvt_pk_bf16_f32 v62, v248, v249\n" \
  "v_cvt_pk_bf16_f32 v63, v250, v251\n" \
  "v_fmamk_f32 v237, v34, 0x41000000, v38\n" \
  "v_fmamk_f32 v239, v34, 0x42200000, v38\n" \
  "v_fmamk_f32 v241, v34, 0x42900000, v38\n" \
  "v_mfma_f32_16x16x32_bf16 a[20:23], v[200:203], v[48:51], 0\n" \
  "buffer_load_dwordx4 a[172:175], v24, s[36:39], s15 offen offset:768 sc1\n" \
  "v_fmamk_f32 v243, v34, 0x42d00000, v38\n" \
  "v_fmamk_f32 v245, v35, 0x41000000, v39\n" \
  "v_fmamk_f32 v247, v35, 0x42200000, v39\n" \
  "v_fmamk_f32 v249, v35, 0x42900000, v39\n" \
  "v_fmamk_f32 v251, v35, 0x42d00000, v39\n" \
  "v_mfma_f32_16x16x32_bf16 a[24:27], v[204:207], v[48:51], 0\n" \
  "v_cos_f32 v236, v237\n" \
  "v_cos_f32 v238, v239\n" \
  "v_cos_f32 v240, v241\n" \
  "v_mfma_f32_16x16x32_bf16 a[28:31], v[208:211], v[48:51], 0\n" \
  "s_mov_b32 s15, 0x180b000\n" \
  "buffer_load_dwordx4 a[176:179], v24, s[36:39], s15 offen sc1\n" \
  "v_cos_f32 v242, v243\n" \
  "v_cos_f32 v244, v245\n" \
  "v_mfma_f32_16x16x32_bf16 a[32:35], v[196:199], v[52:55], 0\n" \
  "v_cos_f32 v246, v247\n" \
  "v_cos_f32 v248, v249\n" \
  "v_cos_f32 v250, v251\n" \
  "v_mfma_f32_16x16x32_bf16 a[36:39], v[200:203], v[52:55], 0\n" \
  "buffer_load_dwordx4 v[88:91], v24, s[36:39], s15 offen offset:256 sc1\n" \
  "v_sin_f32 v237, -v237\n" \
  "v_sin_f32 v239, -v239\n" \
  "v_sin_f32 v241, -v241\n" \
  "v_mfma_f32_16x16x32_bf16 a[40:43], v[204:207], v[52:55], 0\n" \
  "v_sin_f32 v243, -v243\n" \
  "v_sin_f32 v245, -v245\n" \
  "v_sin_f32 v247, -v247\n" \
  "v_mfma_f32_16x16x32_bf16 a[44:47], v[208:211], v[52:55], 0\n" \
  "buffer_load_dwordx4 v[92:95], v24, s[36:39], s15 offen offset:512 sc1\n" \
  "v_sin_f32 v249, -v249\n" \
  "v_sin_f32 v251, -v251\n" \
  "v_cvt_pk_bf16_f32 v64, v236, v237\n" \
  "v_cvt_pk_bf16_f32 v65, v238, v239\n" \
  "v_cvt_pk_bf16_f32 v66, v240, v241\n" \
  "v_cvt_pk_bf16_f32 v67, v242, v243\n" \
  "v_cvt_pk_bf16_f32 v68, v244, v245\n" \
  "v_cvt_pk_bf16_f32 v69, v246, v247\n" \
  "v_cvt_pk_bf16_f32 v70, v248, v249\n" \
  "buffer_load_dwordx4 v[96:99], v24, s[36:39], s15 offen offset:768 sc1\n" \
  "v_cvt_pk_bf16_f32 v71, v250, v251\n" \
  "v_accvgpr_read_b32 v72, a188\n" \
  "v_accvgpr_read_b32 v73, a192\n" \
  "v_accvgpr_read_b32 v74, a196\n" \
  "v_accvgpr_read_b32 v75, a200\n" \
  "v_accvgpr_read_b32 v76, a189\n" \
  "v_accvgpr_read_b32 v77, a193\n" \
  "v_accvgpr_read_b32 v78, a197\n" \
  "v_accvgpr_read_b32 v79, a201\n" \
  "v_accvgpr_read_b32 v80, a190\n" \
  "v_accvgpr_read_b32 v81, a194\n" \
  "v_accvgpr_read_b32 v82, a198\n" \
  "v_accvgpr_read_b32 v83, a202\n" \
  "v_accvgpr_read_b32 v84, a191\n" \
  "v_accvgpr_read_b32 v85, a195\n" \
  "s_mov_b32 s15, 0x180c000\n" \
  "buffer_load_dwordx4 v[100:103], v24, s[36:39], s15 offen sc1\n" \
  "v_accvgpr_read_b32 v86, a199\n" \
  "v_accvgpr_read_b32 v87, a203\n" \
  "buffer_load_dwordx2 v[26:27], v13, s[36:39], 0 offen sc1\n" \
  "v_mfma_f32_16x16x32_bf16 a[88:91], v[212:215], v[72:75], a[88:91]\n" \
  "v_fmamk_f32 v237, v32, 0x41400000, v36\n" \
  "v_fmamk_f32 v239, v32, 0x42300000, v36\n" \
  "v_fmamk_f32 v241, v32, 0x42980000, v36\n" \
  "v_fmamk_f32 v243, v32, 0x42d80000, v36\n" \
  "v_fmamk_f32 v245, v33, 0x41400000, v37\n" \
  "v_fmamk_f32 v247, v33, 0x42300000, v37\n" \
  "v_mfma_f32_16x16x32_bf16 a[92:95], v[216:219], v[72:75], a[92:95]\n" \
  "v_fmamk_f32 v249, v33, 0x42980000, v37\n" \
  "v_fmamk_f32 v251, v33, 0x42d80000, v37\n" \
  "v_cos_f32 v236, v237\n" \
  "v_cos_f32 v238, v239\n" \
  "v_mfma_f32_16x16x32_bf16 a[116:119], v[220:223], v[72:75], a[116:119]\n" \
  "buffer_load_dwordx4 a[188:191], v24, s[36:39], s15 offen offset:256 sc1\n" \
  "v_cos_f32 v240, v241\n" \
  "v_cos_f32 v242, v243\n" \
  "v_cos_f32 v244, v245\n" \
  "v_mfma_f32_16x16x32_bf16 a[120:123], v[224:227], v[72:75], a[120:123]\n" \
  "v_cos_f32 v246, v247\n" \
  "v_cos_f32 v248, v249\n" \
  "v_cos_f32 v250, v251\n" \
  "v_mfma_f32_16x16x32_bf16 a[0:3], v[212:215], v[76:79], a[0:3]\n" \
  "buffer_load_dwordx4 a[192:195], v24, s[36:39], s15 offen offset:512 sc1\n" \
  "v_sin_f32 v237, -v237\n" \
  "v_sin_f32 v239, -v239\n" \
  "v_sin_f32 v241, -v241\n" \
  "v_mfma_f32_16x16x32_bf16 a[4:7], v[216:219], v[76:79], a[4:7]\n" \
  "buffer_load_dwordx4 a[196:199], v24, s[36:39], s15 offen offset:768 sc1\n" \
  "v_sin_f32 v243, -v243\n" \
  "v_sin_f32 v245, -v245\n" \
  "v_sin_f32 v247, -v247\n" \
  "v_mfma_f32_16x16x32_bf16 a[8:11], v[220:223], v[76:79], a[8:11]\n" \
  "v_sin_f32 v249, -v249\n" \
  "v_sin_f32 v251, -v251\n" \
  "v_cvt_pk_bf16_f32 v104, v236, v237\n" \
  "v_cvt_pk_bf16_f32 v105, v238, v239\n" \
  "v_mfma_f32_16x16x32_bf16 a[12:15], v[224:227], v[76:79], a[12:15]\n" \
  "s_mov_b32 s15, 0x180d000\n" \
  "buffer_load_dwordx4 a[200:203], v24, s[36:39], s15 offen sc1\n" \
  "v_cvt_pk_bf16_f32 v106, v240, v241\n" \
  "v_cvt_pk_bf16_f32 v107, v242, v243\n" \
  "v_cvt_pk_bf16_f32 v108, v244, v245\n" \
  "v_cvt_pk_bf16_f32 v109, v246, v247\n" \
  "v_mfma_f32_16x16x32_bf16 a[16:19], v[212:215], v[80:83], a[16:19]\n" \
  "v_cvt_pk_bf16_f32 v110, v248, v249\n" \
  "v_cvt_pk_bf16_f32 v111, v250, v251\n" \
  "v_fmamk_f32 v237, v34, 0x41400000, v38\n" \
  "v_fmamk_f32 v239, v34, 0x42300000, v38\n" \
  "v_fmamk_f32 v241, v34, 0x42980000, v38\n" \
  "v_fmamk_f32 v243, v34, 0x42d80000, v38\n" \
  "v_mfma_f32_16x16x32_bf16 a[20:23], v[216:219], v[80:83], a[20:23]\n" \
  "buffer_load_dwordx4 v[136:139], v24, s[36:39], s15 offen offset:256 sc1\n" \
  "v_fmamk_f32 v245, v35, 0x41400000, v39\n" \
  "v_fmamk_f32 v247, v35, 0x42300000, v39\n" \
  "v_fmamk_f32 v249, v35, 0x42980000, v39\n" \
  "v_fmamk_f32 v251, v35, 0x42d80000, v39\n" \
  "v_cos_f32 v236, v237\n" \
  "v_mfma_f32_16x16x32_bf16 a[24:27], v[220:223], v[80:83], a[24:27]\n" \
  "v_cos_f32 v238, v239\n" \
  "v_cos_f32 v240, v241\n" \
  "v_cos_f32 v242, v243\n" \
  "v_mfma_f32_16x16x32_bf16 a[28:31], v[224:227], v[80:83], a[28:31]\n" \
  "buffer_load_dwordx4 v[140:143], v24, s[36:39], s15 offen offset:512 sc1\n" \
  "v_cos_f32 v244, v245\n" \
  "v_cos_f32 v246, v247\n" \
  "v_cos_f32 v248, v249\n" \
  "v_mfma_f32_16x16x32_bf16 a[32:35], v[212:215], v[84:87], a[32:35]\n" \
  "v_cos_f32 v250, v251\n" \
  "v_sin_f32 v237, -v237\n" \
  "v_sin_f32 v239, -v239\n" \
  "v_mfma_f32_16x16x32_bf16 a[36:39], v[216:219], v[84:87], a[36:39]\n" \
  "v_sin_f32 v241, -v241\n" \
  "v_sin_f32 v243, -v243\n" \
  "v_sin_f32 v245, -v245\n" \
  "v_mfma_f32_16x16x32_bf16 a[40:43], v[220:223], v[84:87], a[40:43]\n" \
  "v_sin_f32 v247, -v247\n" \
  "v_sin_f32 v249, -v249\n" \
  "v_sin_f32 v251, -v251\n" \
  "v_mfma_f32_16x16x32_bf16 a[44:47], v[224:227], v[84:87], a[44:47]\n" \
  "v_cvt_pk_bf16_f32 v112, v236, v237\n" \
  "v_cvt_pk_bf16_f32 v113, v238, v239\n" \
  "v_cvt_pk_bf16_f32 v114, v240, v241\n" \
  "v_cvt_pk_bf16_f32 v115, v242, v243\n" \
  "v_cvt_pk_bf16_f32 v116, v244, v245\n" \
  "v_cvt_pk_bf16_f32 v117, v246, v247\n" \
  "v_cvt_pk_bf16_f32 v118, v248, v249\n" \
  "v_cvt_pk_bf16_f32 v119, v250, v251\n" \
  "v_accvgpr_read_b32 v120, a204\n" \
  "v_accvgpr_read_b32 v121, a208\n" \
  "v_accvgpr_read_b32 v122, a212\n" \
  "v_accvgpr_read_b32 v123, a216\n" \
  "v_accvgpr_read_b32 v124, a205\n" \
  "v_accvgpr_read_b32 v125, a209\n" \
  "v_accvgpr_read_b32 v126, a213\n" \
  "v_accvgpr_read_b32 v127, a217\n" \
  "v_accvgpr_read_b32 v128, a206\n" \
  "v_accvgpr_read_b32 v129, a210\n" \
  "v_accvgpr_read_b32 v130, a214\n" \
  "v_accvgpr_read_b32 v131, a218\n" \
  "v_accvgpr_read_b32 v132, a207\n" \
  "v_accvgpr_read_b32 v133, a211\n" \
  "v_accvgpr_read_b32 v134, a215\n" \
  "v_accvgpr_read_b32 v135, a219\n" \
  "s_waitcnt vmcnt(6)\n" \
  "s_add_u32 s18, s51, 0x10000\n" \
  "buffer_store_dwordx2 v[10:11], v12, s[36:39], s18 offen sc1\n" \
  "s_mov_b32 s18, 0x10000\n" \
  "v_cmp_eq_u64 vcc, s[48:49], v[26:27]\n" \
  "s_nop 0\n" \
  "s_cmp_eq_u64 vcc, -1\n" \
  "s_cbranch_scc0 LA_slow%=\n" \
  "LA_ok%=:\n" \
  "v_mfma_f32_16x16x32_bf16 a[88:91], v[56:59], v[120:123], a[88:91]\n" \
  "buffer_load_dwordx4 a[204:207], v24, s[36:39], s15 offen offset:768 sc1\n" \
  "v_accvgpr_read_b32 v40, a236\n" \
  "v_accvgpr_read_b32 v41, a240\n" \
  "v_accvgpr_read_b32 v42, a244\n" \
  "v_accvgpr_read_b32 v43, a248\n" \
  "v_accvgpr_read_b32 v44, a237\n" \
  "v_mfma_f32_16x16x32_bf16 a[92:95], v[60:63], v[120:123], a[92:95]\n" \
  "s_add_u32 s17, s52, 0x0\n" \
  "buffer_load_dwordx4 a[208:211], v22, s[36:39], s17 offen sc1\n" \
  "v_accvgpr_read_b32 v45, a241\n" \
  "v_accvgpr_read_b32 v46, a245\n" \
  "v_accvgpr_read_b32 v47, a249\n" \
  "v_accvgpr_read_b32 v48, a238\n" \
  "v_mfma_f32_16x16x32_bf16 a[116:119], v[64:67], v[120:123], a[116:119]\n" \
  "buffer_load_dwordx4 a[212:215], v14, s[36:39], s17 offen sc1\n" \
  "v_accvgpr_read_b32 v49, a242\n" \
  "v_accvgpr_read_b32 v50, a246\n" \
  "v_accvgpr_read_b32 v51, a250\n" \
  "v_accvgpr_read_b32 v52, a239\n" \
  "v_accvgpr_read_b32 v53, a243\n" \
  "v_mfma_f32_16x16x32_bf16 a[120:123], v[68:71], v[120:123], a[120:123]\n" \
  "buffer_load_dwordx4 a[216:219], v22, s[36:39], s17 offen offset:1024 sc1\n" \
  "v_accvgpr_read_b32 v54, a247\n" \
  "v_accvgpr_read_b32 v55, a251\n" \
  "v_mfma_f32_16x16x32_bf16 a[0:3], v[56:59], v[124:127], a[0:3]\n" \
  "v_mfma_f32_16x16x32_bf16 a[4:7], v[60:63], v[124:127], a[4:7]\n" \
  "v_mfma_f32_16x16x32_bf16 a[8:11], v[64:67], v[124:127], a[8:11]\n" \
  "v_mfma_f32_16x16x32_bf16 a[12:15], v[68:71], v[124:127], a[12:15]\n" \
  "buffer_load_dwordx4 v[196:199], v14, s[36:39], s17 offen offset:1024 sc1\n" \
  "v_mfma_f32_16x16x32_bf16 a[16:19], v[56:59], v[128:131], a[16:19]\n" \
  "v_mfma_f32_16x16x32_bf16 a[20:23], v[60:63], v[128:131], a[20:23]\n" \
  "v_mfma_f32_16x16x32_bf16 a[24:27], v[64:67], v[128:131], a[24:27]\n" \
  "v_mfma_f32_16x16x32_bf16 a[28:31], v[68:71], v[128:131], a[28:31]\n" \
  "buffer_load_dwordx4 v[200:203], v22, s[36:39], s17 offen offset:2048 sc1\n" \
  "v_mfma_f32_16x16x32_bf16 a[32:35], v[56:59], v[132:135], a[32:35]\n" \
  "v_mfma_f32_16x16x32_bf16 a[36:39], v[60:63], v[132:135], a[36:39]\n" \
  "v_mfma_f32_16x16x32_bf16 a[40:43], v[64:67], v[132:135], a[40:43]\n" \
  "v_mfma_f32_16x16x32_bf16 a[44:47], v[68:71], v[132:135], a[44:47]\n" \
  "buffer_load_dwordx4 v[204:207], v14, s[36:39], s17 offen offset:2048 sc1\n" \
  "v_mfma_f32_16x16x32_bf16 a[88:91], v[104:107], v[40:43], a[88:91]\n" \
  "v_mfma_f32_16x16x32_bf16 a[92:95], v[108:111], v[40:43], a[92:95]\n" \
  "v_mfma_f32_16x16x32_bf16 a[116:119], v[112:115], v[40:43], a[116:119]\n" \
  "v_mfma_f32_16x16x32_bf16 a[120:123], v[116:119], v[40:43], a[120:123]\n" \
  "buffer_load_dwordx4 a[236:239], v22, s[36:39], s17 offen offset:3072 sc1\n" \
  "v_mfma_f32_16x16x32_bf16 a[0:3], v[104:107], v[44:47], a[0:3]\n" \
  "v_mfma_f32_16x16x32_bf16 a[4:7], v[108:111], v[44:47], a[4:7]\n" \
  "v_mfma_f32_16x16x32_bf16 a[8:11], v[112:115], v[44:47], a[8:11]\n" \
  "v_mfma_f32_16x16x32_bf16 a[12:15], v[116:119], v[44:47], a[12:15]\n" \
  "buffer_load_dwordx4 a[240:243], v14, s[36:39], s17 offen offset:3072 sc1\n" \
  "v_mfma_f32_16x16x32_bf16 a[16:19], v[104:107], v[48:51], a[16:19]\n" \
  "v_mfma_f32_16x16x32_bf16 a[20:23], v[108:111], v[48:51], a[20:23]\n" \
  "v_mfma_f32_16x16x32_bf16 a[24:27], v[112:115], v[48:51], a[24:27]\n" \
  "v_mfma_f32_16x16x32_bf16 a[28:31], v[116:119], v[48:51], a[28:31]\n" \
  "s_add_u32 s17, s52, 0x1000\n" \
  "buffer_load_dwordx4 a[244:247], v22, s[36:39], s17 offen sc1\n" \
  "v_mfma_f32_16x16x32_bf16 a[32:35], v[104:107], v[52:55], a[32:35]\n" \
  "v_mfma_f32_16x16x32_bf16 a[36:39], v[108:111], v[52:55], a[36:39]\n" \
  "v_mfma_f32_16x16x32_bf16 a[40:43], v[112:115], v[52:55], a[40:43]\n" \
  "v_mfma_f32_16x16x32_bf16 a[44:47], v[116:119], v[52:55], a[44:47]\n" \
  "buffer_load_dwordx4 a[248:251], v14, s[36:39], s17 offen sc1\n" \
  "v_add_u32 v17, 0x1c000, v31\n" \
  "v_add_u32 v8, 0x1c000, v9\n" \
  "s_waitcnt vmcnt(9)\n" \
  "s_waitcnt vmcnt(63)\n" \
  "v_mfma_f32_16x16x32_bf16 a[88:91], a[144:147], a[208:211], a[88:91]\n" \
  "v_mfma_f32_16x16x32_bf16 a[92:95], a[148:151], a[208:211], a[92:95]\n" \
  "v_mfma_f32_16x16x32_bf16 a[116:119], a[152:155], a[208:211], a[116:119]\n" \
  "v_mfma_f32_16x16x32_bf16 a[120:123], a[156:159], a[208:211], a[120:123]\n" \
  "buffer_load_dwordx4 v[208:211], v22, s[36:39], s17 offen offset:1024 sc1\n" \
  "s_waitcnt vmcnt(9)\n" \
  "v_mfma_f32_16x16x32_bf16 a[12:15], a[156:159], a[212:215], a[12:15]\n" \
  "v_mfma_f32_16x16x32_bf16 a[8:11], a[152:155], a[212:215], a[8:11]\n" \
  "v_mfma_f32_16x16x32_bf16 a[4:7], a[148:151], a[212:215], a[4:7]\n" \
  "v_mfma_f32_16x16x32_bf16 a[0:3], a[144:147], a[212:215], a[0:3]\n" \
  "buffer_load_dwordx4 v[72:75], v14, s[36:39], s17 offen offset:1024 sc1\n" \
  "s_waitcnt vmcnt(9)\n" \
  "s_waitcnt vmcnt(63)\n" \
  "v_mfma_f32_16x16x32_bf16 a[88:91], a[160:163], a[216:219], a[88:91]\n" \
  "v_mfma_f32_16x16x32_bf16 a[92:95], a[252:255], a[216:219], a[92:95]\n" \
  "v_mfma_f32_16x16x32_bf16 a[116:119], a[72:75], a[216:219], a[116:119]\n" \
  "v_mfma_f32_16x16x32_bf16 a[120:123], a[76:79], a[216:219], a[120:123]\n" \
  "buffer_load_dwordx4 a[208:211], v22, s[36:39], s17 offen offset:2048 sc1\n" \
  "s_waitcnt vmcnt(9)\n" \
  "v_mfma_f32_16x16x32_bf16 a[12:15], a[76:79], v[196:199], a[12:15]\n" \
  "v_mfma_f32_16x16x32_bf16 a[8:11], a[72:75], v[196:199], a[8:11]\n" \
  "v_mfma_f32_16x16x32_bf16 a[4:7], a[252:255], v[196:199], a[4:7]\n" \
  "v_mfma_f32_16x16x32_bf16 a[0:3], a[160:163], v[196:199], a[0:3]\n" \
  "buffer_load_dwordx4 a[212:215], v14, s[36:39], s17 offen offset:2048 sc1\n" \
  "s_waitcnt vmcnt(9)\n" \
  "s_waitcnt vmcnt(63)\n" \
  "v_mfma_f32_16x16x32_bf16 a[88:91], a[80:83], v[200:203], a[88:91]\n" \
  "buffer_load_dwordx2 v[26:27], v13, s[36:39], s18 offen sc1\n" \
  "v_mfma_f32_16x16x32_bf16 a[92:95], a[84:87], v[200:203], a[92:95]\n" \
  "v_mfma_f32_16x16x32_bf16 a[116:119], a[96:99], v[200:203], a[116:119]\n" \
  "v_mfma_f32_16x16x32_bf16 a[120:123], a[100:103], v[200:203], a[120:123]\n" \
  "buffer_load_dwordx4 a[216:219], v22, s[36:39], s17 offen offset:3072 sc1\n" \
  "s_waitcnt vmcnt(10)\n" \
  "v_mfma_f32_16x16x32_bf16 a[12:15], a[100:103], v[204:207], a[12:15]\n" \
  "v_mfma_f32_16x16x32_bf16 a[8:11], a[96:99], v[204:207], a[8:11]\n" \
  "v_mfma_f32_16x16x32_bf16 a[4:7], a[84:87], v[204:207], a[4:7]\n" \
  "v_mfma_f32_16x16x32_bf16 a[0:3], a[80:83], v[204:207], a[0:3]\n" \
  "buffer_load_dwordx4 v[76:79], v14, s[36:39], s17 offen offset:3072 sc1\n" \
  "s_waitcnt vmcnt(10)\n" \
  "s_waitcnt vmcnt(63)\n" \
  "v_mfma_f32_16x16x32_bf16 a[88:91], a[104:107], a[236:239], a[88:91]\n" \
  "v_mfma_f32_16x16x32_bf16 a[92:95], a[108:111], a[236:239], a[92:95]\n" \
  "v_mfma_f32_16x16x32_bf16 a[116:119], a[112:115], a[236:239], a[116:119]\n" \
  "v_mfma_f32_16x16x32_bf16 a[120:123], a[124:127], a[236:239], a[120:123]\n" \
  "s_add_u32 s17, s52, 0x2000\n" \
  "buffer_load_dwordx4 v[80:83], v22, s[36:39], s17 offen sc1\n" \
  "s_waitcnt vmcnt(10)\n" \
  "v_mfma_f32_16x16x32_bf16 a[12:15], a[124:127], a[240:243], a[12:15]\n" \
  "v_mfma_f32_16x16x32_bf16 a[8:11], a[112:115], a[240:243], a[8:11]\n" \
  "v_mfma_f32_16x16x32_bf16 a[4:7], a[108:111], a[240:243], a[4:7]\n" \
  "v_mfma_f32_16x16x32_bf16 a[0:3], a[104:107], a[240:243], a[0:3]\n" \
  "buffer_load_dwordx4 v[84:87], v14, s[36:39], s17 offen sc1\n" \
  "s_waitcnt vmcnt(4)\n" \
  "v_cmp_eq_u64 vcc, s[48:49], v[26:27]\n" \
  "s_nop 0\n" \
  "s_cmp_eq_u64 vcc, -1\n" \
  "s_cbranch_scc0 LB_slow%=\n" \
  "LB_ok%=:\n" \
  "s_waitcnt vmcnt(10)\n" \
  "s_waitcnt vmcnt(63)\n" \
  "v_mfma_f32_16x16x32_bf16 a[88:91], a[128:131], a[244:247], a[88:91]\n" \
  "v_mfma_f32_16x16x32_bf16 a[92:95], a[132:135], a[244:247], a[92:95]\n" \
  "v_mfma_f32_16x16x32_bf16 a[116:119], v[228:231], a[244:247], a[116:119]\n" \
  "buffer_load_dwordx4 a[236:239], v22, s[36:39], s17 offen offset:1024 sc1\n" \
  "v_mfma_f32_16x16x32_bf16 a[120:123], a[56:59], a[244:247], a[120:123]\n" \
  "s_waitcnt vmcnt(10)\n" \
  "v_mfma_f32_16x16x32_bf16 a[12:15], a[56:59], a[248:251], a[12:15]\n" \
  "v_mfma_f32_16x16x32_bf16 a[8:11], v[228:231], a[248:251], a[8:11]\n" \
  "v_mfma_f32_16x16x32_bf16 a[4:7], a[132:135], a[248:251], a[4:7]\n" \
  "buffer_load_dwordx4 a[240:243], v14, s[36:39], s17 offen offset:1024 sc1\n" \
  "v_mfma_f32_16x16x32_bf16 a[0:3], a[128:131], a[248:251], a[0:3]\n" \
  "s_waitcnt vmcnt(10)\n" \
  "s_waitcnt vmcnt(63)\n" \
  "v_mfma_f32_16x16x32_bf16 a[88:91], a[60:63], v[208:211], a[88:91]\n" \
  "v_mfma_f32_16x16x32_bf16 a[92:95], a[64:67], v[208:211], a[92:95]\n" \
  "v_mfma_f32_16x16x32_bf16 a[116:119], a[68:71], v[208:211], a[116:119]\n" \
  "buffer_load_dwordx4 a[244:247], v22, s[36:39], s17 offen offset:2048 sc1\n" \
  "v_mfma_f32_16x16x32_bf16 a[120:123], v[232:235], v[208:211], a[120:123]\n" \
  "s_waitcnt vmcnt(10)\n" \
  "v_mfma_f32_16x16x32_bf16 a[12:15], v[232:235], v[72:75], a[12:15]\n" \
  "v_mfma_f32_16x16x32_bf16 a[8:11], a[68:71], v[72:75], a[8:11]\n" \
  "v_mfma_f32_16x16x32_bf16 a[4:7], a[64:67], v[72:75], a[4:7]\n" \
  "buffer_load_dwordx4 a[248:251], v14, s[36:39], s17 offen offset:2048 sc1\n" \
  "v_mfma_f32_16x16x32_bf16 a[0:3], a[60:63], v[72:75], a[0:3]\n" \
  "s_waitcnt vmcnt(10)\n" \
  "s_waitcnt vmcnt(62)\n" \
  "v_mfma_f32_16x16x32_bf16 a[88:91], v[176:179], a[208:211], a[88:91]\n" \
  "v_mfma_f32_16x16x32_bf16 a[92:95], v[180:183], a[208:211], a[92:95]\n" \
  "v_mfma_f32_16x16x32_bf16 a[116:119], v[184:187], a[208:211], a[116:119]\n" \
  "buffer_load_dwordx4 v[212:215], v22, s[36:39], s17 offen offset:3072 sc1\n" \
  "v_mfma_f32_16x16x32_bf16 a[120:123], v[188:191], a[208:211], a[120:123]\n" \
  "s_waitcnt vmcnt(10)\n" \
  "v_mfma_f32_16x16x32_bf16 a[12:15], v[188:191], a[212:215], a[12:15]\n" \
  "v_mfma_f32_16x16x32_bf16 a[8:11], v[184:187], a[212:215], a[8:11]\n" \
  "v_mfma_f32_16x16x32_bf16 a[4:7], v[180:183], a[212:215], a[4:7]\n" \
  "buffer_load_dwordx4 v[216:219], v14, s[36:39], s17 offen offset:3072 sc1\n" \
  "v_mfma_f32_16x16x32_bf16 a[0:3], v[176:179], a[212:215], a[0:3]\n" \
  "s_waitcnt vmcnt(9)\n" \
  "s_waitcnt vmcnt(60)\n" \
  "v_mfma_f32_16x16x32_bf16 a[88:91], v[192:195], a[216:219], a[88:91]\n" \
  "v_mfma_f32_16x16x32_bf16 a[92:95], a[180:183], a[216:219], a[92:95]\n" \
  "v_mfma_f32_16x16x32_bf16 a[116:119], a[184:187], a[216:219], a[116:119]\n" \
  "s_add_u32 s17, s52, 0x3000\n" \
  "buffer_load_dwordx4 a[208:211], v22, s[36:39], s17 offen sc1\n" \
  "v_mfma_f32_16x16x32_bf16 a[120:123], a[220:223], a[216:219], a[120:123]\n" \
  "s_waitcnt vmcnt(9)\n" \
  "v_mfma_f32_16x16x32_bf16 a[12:15], a[220:223], v[76:79], a[12:15]\n" \
  "v_mfma_f32_16x16x32_bf16 a[8:11], a[184:187], v[76:79], a[8:11]\n" \
  "v_mfma_f32_16x16x32_bf16 a[4:7], a[180:183], v[76:79], a[4:7]\n" \
  "buffer_load_dwordx4 a[212:215], v14, s[36:39], s17 offen sc1\n" \
  "v_mfma_f32_16x16x32_bf16 a[0:3], v[192:195], v[76:79], a[0:3]\n" \
  "s_waitcnt vmcnt(9)\n" \
  "s_waitcnt vmcnt(58)\n" \
  "v_mfma_f32_16x16x32_bf16 a[88:91], a[232:235], v[80:83], a[88:91]\n" \
  "v_mfma_f32_16x16x32_bf16 a[92:95], v[144:147], v[80:83], a[92:95]\n" \
  "v_mfma_f32_16x16x32_bf16 a[116:119], v[148:151], v[80:83], a[116:119]\n" \
  "buffer_load_dwordx4 a[216:219], v22, s[36:39], s17 offen offset:1024 sc1\n" \
  "v_mfma_f32_16x16x32_bf16 a[120:123], v[152:155], v[80:83], a[120:123]\n" \
  "s_waitcnt vmcnt(9)\n" \
  "v_mfma_f32_16x16x32_bf16 a[12:15], v[152:155], v[84:87], a[12:15]\n" \
  "v_mfma_f32_16x16x32_bf16 a[8:11], v[148:151], v[84:87], a[8:11]\n" \
  "v_mfma_f32_16x16x32_bf16 a[4:7], v[144:147], v[84:87], a[4:7]\n" \
  "buffer_load_dwordx4 v[220:223], v14, s[36:39], s17 offen offset:1024 sc1\n" \
  "v_mfma_f32_16x16x32_bf16 a[0:3], a[232:235], v[84:87], a[0:3]\n" \
  "s_waitcnt vmcnt(9)\n" \
  "s_waitcnt vmcnt(47)\n" \
  "v_mfma_f32_16x16x32_bf16 a[88:91], v[156:159], a[236:239], a[88:91]\n" \
  "v_mfma_f32_16x16x32_bf16 a[92:95], a[224:227], a[236:239], a[92:95]\n" \
  "v_mfma_f32_16x16x32_bf16 a[116:119], a[228:231], a[236:239], a[116:119]\n" \
  "s_add_u32 s17, s52, 0x0\n" \
  "buffer_load_dwordx4 v[224:227], v15, s[36:39], s17 offen sc1\n" \
  "v_mfma_f32_16x16x32_bf16 a[120:123], a[48:51], a[236:239], a[120:123]\n" \
  "s_waitcnt vmcnt(9)\n" \
  "v_mfma_f32_16x16x32_bf16 a[12:15], a[48:51], a[240:243], a[12:15]\n" \
  "v_mfma_f32_16x16x32_bf16 a[8:11], a[228:231], a[240:243], a[8:11]\n" \
  "v_mfma_f32_16x16x32_bf16 a[4:7], a[224:227], a[240:243], a[4:7]\n" \
  "buffer_load_dwordx4 v[120:123], v16, s[36:39], s17 offen sc1\n" \
  "v_mfma_f32_16x16x32_bf16 a[0:3], v[156:159], a[240:243], a[0:3]\n" \
  "s_waitcnt vmcnt(9)\n" \
  "s_waitcnt vmcnt(45)\n" \
  "v_mfma_f32_16x16x32_bf16 a[88:91], a[52:55], a[244:247], a[88:91]\n" \
  "v_mfma_f32_16x16x32_bf16 a[92:95], a[164:167], a[244:247], a[92:95]\n" \
  "v_mfma_f32_16x16x32_bf16 a[116:119], a[168:171], a[244:247], a[116:119]\n" \
  "buffer_load_dwordx4 a[236:239], v15, s[36:39], s17 offen offset:1024 sc1\n" \
  "v_mfma_f32_16x16x32_bf16 a[120:123], a[172:175], a[244:247], a[120:123]\n" \
  "s_waitcnt vmcnt(9)\n" \
  "v_mfma_f32_16x16x32_bf16 a[12:15], a[172:175], a[248:251], a[12:15]\n" \
  "v_mfma_f32_16x16x32_bf16 a[8:11], a[168:171], a[248:251], a[8:11]\n" \
  "v_mfma_f32_16x16x32_bf16 a[4:7], a[164:167], a[248:251], a[4:7]\n" \
  "buffer_load_dwordx4 a[240:243], v16, s[36:39], s17 offen offset:1024 sc1\n" \
  "v_mfma_f32_16x16x32_bf16 a[0:3], a[52:55], a[248:251], a[0:3]\n" \
  "s_waitcnt vmcnt(9)\n" \
  "s_waitcnt vmcnt(43)\n" \
  "v_mfma_f32_16x16x32_bf16 a[88:91], a[176:179], v[212:215], a[88:91]\n" \
  "v_mfma_f32_16x16x32_bf16 a[92:95], v[88:91], v[212:215], a[92:95]\n" \
  "v_mfma_f32_16x16x32_bf16 a[116:119], v[92:95], v[212:215], a[116:119]\n" \
  "buffer_load_dwordx4 a[244:247], v15, s[36:39], s17 offen offset:2048 sc1\n" \
  "v_mfma_f32_16x16x32_bf16 a[120:123], v[96:99], v[212:215], a[120:123]\n" \
  "s_waitcnt vmcnt(9)\n" \
  "v_mfma_f32_16x16x32_bf16 a[12:15], v[96:99], v[216:219], a[12:15]\n" \
  "v_mfma_f32_16x16x32_bf16 a[8:11], v[92:95], v[216:219], a[8:11]\n" \
  "v_mfma_f32_16x16x32_bf16 a[4:7], v[88:91], v[216:219], a[4:7]\n" \
  "buffer_load_dwordx4 a[248:251], v16, s[36:39], s17 offen offset:2048 sc1\n" \
  "v_mfma_f32_16x16x32_bf16 a[0:3], a[176:179], v[216:219], a[0:3]\n" \
  "s_waitcnt vmcnt(9)\n" \
  "s_waitcnt vmcnt(40)\n" \
  "v_mfma_f32_16x16x32_bf16 a[88:91], v[100:103], a[208:211], a[88:91]\n" \
  "v_mfma_f32_16x16x32_bf16 a[92:95], a[188:191], a[208:211], a[92:95]\n" \
  "v_mfma_f32_16x16x32_bf16 a[116:119], a[192:195], a[208:211], a[116:119]\n" \
  "buffer_load_dwordx4 v[124:127], v15, s[36:39], s17 offen offset:3072 sc1\n" \
  "v_mfma_f32_16x16x32_bf16 a[120:123], a[196:199], a[208:211], a[120:123]\n" \
  "s_waitcnt vmcnt(9)\n" \
  "v_mfma_f32_16x16x32_bf16 a[12:15], a[196:199], a[212:215], a[12:15]\n" \
  "v_mfma_f32_16x16x32_bf16 a[8:11], a[192:195], a[212:215], a[8:11]\n" \
  "v_mfma_f32_16x16x32_bf16 a[4:7], a[188:191], a[212:215], a[4:7]\n" \
  "buffer_load_dwordx4 v[128:131], v16, s[36:39], s17 offen offset:3072 sc1\n" \
  "v_mfma_f32_16x16x32_bf16 a[0:3], v[100:103], a[212:215], a[0:3]\n" \
  "s_waitcnt vmcnt(9)\n" \
  "s_waitcnt vmcnt(37)\n" \
  "v_mfma_f32_16x16x32_bf16 a[88:91], a[200:203], a[216:219], a[88:91]\n" \
  "v_mfma_f32_16x16x32_bf16 a[92:95], v[136:139], a[216:219], a[92:95]\n" \
  "v_mfma_f32_16x16x32_bf16 a[116:119], v[140:143], a[216:219], a[116:119]\n" \
  "s_add_u32 s17, s52, 0x1000\n" \
  "buffer_load_dwordx4 a[208:211], v15, s[36:39], s17 offen sc1\n" \
  "v_mfma_f32_16x16x32_bf16 a[120:123], a[204:207], a[216:219], a[120:123]\n" \
  "s_waitcnt vmcnt(9)\n" \
  "v_mfma_f32_16x16x32_bf16 a[12:15], a[204:207], v[220:223], a[12:15]\n" \
  "v_mfma_f32_16x16x32_bf16 a[8:11], v[140:143], v[220:223], a[8:11]\n" \
  "v_mfma_f32_16x16x32_bf16 a[4:7], v[136:139], v[220:223], a[4:7]\n" \
  "buffer_load_dwordx4 a[212:215], v16, s[36:39], s17 offen sc1\n" \
  "v_mfma_f32_16x16x32_bf16 a[0:3], a[200:203], v[220:223], a[0:3]\n" \
  "s_waitcnt vmcnt(9)\n" \
  "v_mfma_f32_16x16x32_bf16 a[16:19], a[144:147], v[224:227], a[16:19]\n" \
  "v_mfma_f32_16x16x32_bf16 a[20:23], a[148:151], v[224:227], a[20:23]\n" \
  "v_mfma_f32_16x16x32_bf16 a[24:27], a[152:155], v[224:227], a[24:27]\n" \
  "buffer_load_dwordx4 a[216:219], v15, s[36:39], s17 offen offset:1024 sc1\n" \
  "ds_write_b128 v17, a[88:91]\n" \
  "v_mfma_f32_16x16x32_bf16 a[28:31], a[156:159], v[224:227], a[28:31]\n" \
  "ds_write_b128 v17, a[92:95] offset:1024\n" \
  "ds_write_b128 v17, a[116:119] offset:8192\n" \
  "s_waitcnt vmcnt(9)\n" \
  "v_mfma_f32_16x16x32_bf16 a[44:47], a[156:159], v[120:123], a[44:47]\n" \
  "ds_write_b128 v17, a[120:123] offset:9216\n" \
  "ds_write_b128 v17, a[0:3] offset:16384\n" \
  "v_mfma_f32_16x16x32_bf16 a[40:43], a[152:155], v[120:123], a[40:43]\n" \
  "ds_write_b128 v17, a[4:7] offset:17408\n" \
  "ds_write_b128 v17, a[8:11] offset:24576\n" \
  "v_mfma_f32_16x16x32_bf16 a[36:39], a[148:151], v[120:123], a[36:39]\n" \
  "buffer_load_dwordx4 v[132:135], v16, s[36:39], s17 offen offset:1024 sc1\n" \
  "ds_write_b128 v17, a[12:15] offset:25600\n" \
  "v_mfma_f32_16x16x32_bf16 a[32:35], a[144:147], v[120:123], a[32:35]\n" \
  "s_waitcnt vmcnt(9)\n" \
  "v_mfma_f32_16x16x32_bf16 a[16:19], a[160:163], a[236:239], a[16:19]\n" \
  "v_mfma_f32_16x16x32_bf16 a[20:23], a[252:255], a[236:239], a[20:23]\n" \
  "v_mfma_f32_16x16x32_bf16 a[24:27], a[72:75], a[236:239], a[24:27]\n" \
  "buffer_load_dwordx4 a[144:147], v15, s[36:39], s17 offen offset:2048 sc1\n" \
  "v_mfma_f32_16x16x32_bf16 a[28:31], a[76:79], a[236:239], a[28:31]\n" \
  "s_waitcnt vmcnt(9)\n" \
  "v_mfma_f32_16x16x32_bf16 a[44:47], a[76:79], a[240:243], a[44:47]\n" \
  "v_mfma_f32_16x16x32_bf16 a[40:43], a[72:75], a[240:243], a[40:43]\n" \
  "v_mfma_f32_16x16x32_bf16 a[36:39], a[252:255], a[240:243], a[36:39]\n" \
  "buffer_load_dwordx4 a[148:151], v16, s[36:39], s17 offen offset:2048 sc1\n" \
  "v_mfma_f32_16x16x32_bf16 a[32:35], a[160:163], a[240:243], a[32:35]\n" \
  "s_waitcnt vmcnt(9)\n" \
  "v_mfma_f32_16x16x32_bf16 a[16:19], a[80:83], a[244:247], a[16:19]\n" \
  "v_mfma_f32_16x16x32_bf16 a[20:23], a[84:87], a[244:247], a[20:23]\n" \
  "v_mfma_f32_16x16x32_bf16 a[24:27], a[96:99], a[244:247], a[24:27]\n" \
  "buffer_load_dwordx4 a[152:155], v15, s[36:39], s17 offen offset:3072 sc1\n" \
  "v_mfma_f32_16x16x32_bf16 a[28:31], a[100:103], a[244:247], a[28:31]\n" \
  "s_waitcnt vmcnt(9)\n" \
  "v_mfma_f32_16x16x32_bf16 a[44:47], a[100:103], a[248:251], a[44:47]\n" \
  "v_mfma_f32_16x16x32_bf16 a[40:43], a[96:99], a[248:251], a[40:43]\n" \
  "v_mfma_f32_16x16x32_bf16 a[36:39], a[84:87], a[248:251], a[36:39]\n" \
  "buffer_load_dwordx4 a[156:159], v16, s[36:39], s17 offen offset:3072 sc1\n" \
  "v_mfma_f32_16x16x32_bf16 a[32:35], a[80:83], a[248:251], a[32:35]\n" \
  "s_waitcnt vmcnt(9)\n" \
  "v_mfma_f32_16x16x32_bf16 a[16:19], a[104:107], v[124:127], a[16:19]\n" \
  "v_mfma_f32_16x16x32_bf16 a[20:23], a[108:111], v[124:127], a[20:23]\n" \
  "v_mfma_f32_16x16x32_bf16 a[24:27], a[112:115], v[124:127], a[24:27]\n" \
  "s_add_u32 s17, s52, 0x2000\n" \
  "buffer_load_dwordx4 a[236:239], v15, s[36:39], s17 offen sc1\n" \
  "v_mfma_f32_16x16x32_bf16 a[28:31], a[124:127], v[124:127], a[28:31]\n" \
  "s_waitcnt vmcnt(9)\n" \
  "v_mfma_f32_16x16x32_bf16 a[44:47], a[124:127], v[128:131], a[44:47]\n" \
  "v_mfma_f32_16x16x32_bf16 a[40:43], a[112:115], v[128:131], a[40:43]\n" \
  "v_mfma_f32_16x16x32_bf16 a[36:39], a[108:111], v[128:131], a[36:39]\n" \
  "buffer_load_dwordx4 a[240:243], v16, s[36:39], s17 offen sc1\n" \
  "v_mfma_f32_16x16x32_bf16 a[32:35], a[104:107], v[128:131], a[32:35]\n" \
  "s_waitcnt vmcnt(9)\n" \
  "v_mfma_f32_16x16x32_bf16 a[16:19], a[128:131], a[208:211], a[16:19]\n" \
  "s_waitcnt lgkmcnt(0)\n" \
  "s_barrier\n" \
  "v_mfma_f32_16x16x32_bf16 a[20:23], a[132:135], a[208:211], a[20:23]\n" \
  "ds_read_b128 v[236:239], v8\n" \
  "ds_read_b128 v[240:243], v8 offset:1024\n" \
  "v_mfma_f32_16x16x32_bf16 a[24:27], v[228:231], a[208:211], a[24:27]\n" \
  "buffer_load_dwordx4 a[160:163], v15, s[36:39], s17 offen offset:1024 sc1\n" \
  "ds_read_b128 v[244:247], v8 offset:2048\n" \
  "v_mfma_f32_16x16x32_bf16 a[28:31], a[56:59], a[208:211], a[28:31]\n" \
  "ds_read_b128 v[248:251], v8 offset:3072\n" \
  "ds_read_b128 v[252:255], v8 offset:4096\n" \
  "s_waitcnt vmcnt(9)\n" \
  "v_mfma_f32_16x16x32_bf16 a[44:47], a[56:59], a[212:215], a[44:47]\n" \
  "ds_read_b128 v[164:167], v8 offset:5120\n" \
  "ds_read_b128 v[168:171], v8 offset:6144\n" \
  "v_mfma_f32_16x16x32_bf16 a[40:43], v[228:231], a[212:215], a[40:43]\n" \
  "ds_read_b128 v[172:175], v8 offset:7168\n" \
  "v_accvgpr_read_b32 v33, a136\n" \
  "v_mfma_f32_16x16x32_bf16 a[36:39], a[132:135], a[212:215], a[36:39]\n" \
  "buffer_load_dwordx4 a[252:255], v16, s[36:39], s17 offen offset:1024 sc1\n" \
  "v_accvgpr_read_b32 v35, a137\n" \
  "v_mfma_f32_16x16x32_bf16 a[32:35], a[128:131], a[212:215], a[32:35]\n" \
  "v_accvgpr_read_b32 v37, a138\n" \
  "v_accvgpr_read_b32 v39, a139\n" \
  "v_lshlrev_b32 v32, 16, v33\n" \
  "v_lshlrev_b32 v34, 16, v35\n" \
  "v_lshlrev_b32 v36, 16, v37\n" \
  "v_lshlrev_b32 v38, 16, v39\n" \
  "v_and_b32 v33, 0xffff0000, v33\n" \
  "v_and_b32 v35, 0xffff0000, v35\n" \
  "v_and_b32 v37, 0xffff0000, v37\n" \
  "buffer_load_dwordx4 a[72:75], v15, s[36:39], s17 offen offset:2048 sc1\n" \
  "v_and_b32 v39, 0xffff0000, v39\n" \
  "s_waitcnt vmcnt(10)\n" \
  "v_mfma_f32_16x16x32_bf16 a[16:19], a[60:63], a[216:219], a[16:19]\n" \
  "v_mfma_f32_16x16x32_bf16 a[20:23], a[64:67], a[216:219], a[20:23]\n" \
  "v_mfma_f32_16x16x32_bf16 a[24:27], a[68:71], a[216:219], a[24:27]\n" \
  "v_mfma_f32_16x16x32_bf16 a[28:31], v[232:235], a[216:219], a[28:31]\n" \
  "buffer_load_dwordx4 a[76:79], v16, s[36:39], s17 offen offset:2048 sc1\n" \
  "s_waitcnt vmcnt(10)\n" \
  "v_mfma_f32_16x16x32_bf16 a[44:47], v[232:235], v[132:135], a[44:47]\n" \
  "v_mfma_f32_16x16x32_bf16 a[40:43], a[68:71], v[132:135], a[40:43]\n" \
  "v_mfma_f32_16x16x32_bf16 a[36:39], a[64:67], v[132:135], a[36:39]\n" \
  "v_mfma_f32_16x16x32_bf16 a[32:35], a[60:63], v[132:135], a[32:35]\n" \
  "buffer_load_dwordx4 a[244:247], v15, s[36:39], s17 offen offset:3072 sc1\n" \
  "s_waitcnt vmcnt(10)\n" \
  "v_mfma_f32_16x16x32_bf16 a[16:19], v[176:179], a[144:147], a[16:19]\n" \
  "s_waitcnt lgkmcnt(0)\n" \
  "v_add_f32 v236, v236, v244\n" \
  "v_mfma_f32_16x16x32_bf16 a[20:23], v[180:183], a[144:147], a[20:23]\n" \
  "v_add_f32 v237, v237, v245\n" \
  "v_add_f32 v238, v238, v246\n" \
  "v_mfma_f32_16x16x32_bf16 a[24:27], v[184:187], a[144:147], a[24:27]\n" \
  "v_add_f32 v239, v239, v247\n" \
  "v_add_f32 v252, v252, v168\n" \
  "v_mfma_f32_16x16x32_bf16 a[28:31], v[188:191], a[144:147], a[28:31]\n" \
  "buffer_load_dwordx4 a[248:251], v16, s[36:39], s17 offen offset:3072 sc1\n" \
  "v_add_f32 v253, v253, v169\n" \
  "s_waitcnt vmcnt(10)\n" \
  "v_mfma_f32_16x16x32_bf16 a[44:47], v[188:191], a[148:151], a[44:47]\n" \
  "v_add_f32 v254, v254, v170\n" \
  "v_add_f32 v255, v255, v171\n" \
  "v_mfma_f32_16x16x32_bf16 a[40:43], v[184:187], a[148:151], a[40:43]\n" \
  "v_mfma_f32_16x16x32_bf16 a[36:39], v[180:183], a[148:151], a[36:39]\n" \
  "v_mfma_f32_16x16x32_bf16 a[32:35], v[176:179], a[148:151], a[32:35]\n" \
  "s_add_u32 s17, s52, 0x3000\n" \
  "buffer_load_dwordx4 a[80:83], v15, s[36:39], s17 offen sc1\n" \
  "s_waitcnt vmcnt(10)\n" \
  "v_mfma_f32_16x16x32_bf16 a[16:19], v[192:195], a[152:155], a[16:19]\n" \
  "v_add_f32 v240, v240, v248\n" \
  "v_add_f32 v241, v241, v249\n" \
  "v_mfma_f32_16x16x32_bf16 a[20:23], a[180:183], a[152:155], a[20:23]\n" \
  "v_add_f32 v242, v242, v250\n" \
  "v_add_f32 v243, v243, v251\n" \
  "v_mfma_f32_16x16x32_bf16 a[24:27], a[184:187], a[152:155], a[24:27]\n" \
  "v_add_f32 v164, v164, v172\n" \
  "v_add_f32 v165, v165, v173\n" \
  "v_mfma_f32_16x16x32_bf16 a[28:31], a[220:223], a[152:155], a[28:31]\n" \
  "buffer_load_dwordx4 a[84:87], v16, s[36:39], s17 offen sc1\n" \
  "v_add_f32 v166, v166, v174\n" \
  "s_waitcnt vmcnt(10)\n" \
  "v_mfma_f32_16x16x32_bf16 a[44:47], a[220:223], a[156:159], a[44:47]\n" \
  "v_add_f32 v167, v167, v175\n" \
  "v_add_f32 v236, v236, v32\n" \
  "v_mfma_f32_16x16x32_bf16 a[40:43], a[184:187], a[156:159], a[40:43]\n" \
  "v_mfma_f32_16x16x32_bf16 a[36:39], a[180:183], a[156:159], a[36:39]\n" \
  "buffer_load_dwordx4 a[96:99], v15, s[36:39], s17 offen offset:1024 sc1\n" \
  "v_mfma_f32_16x16x32_bf16 a[32:35], v[192:195], a[156:159], a[32:35]\n" \
  "s_waitcnt vmcnt(10)\n" \
  "v_mfma_f32_16x16x32_bf16 a[16:19], a[232:235], a[236:239], a[16:19]\n" \
  "v_add_f32 v237, v237, v33\n" \
  "v_add_f32 v238, v238, v34\n" \
  "v_mfma_f32_16x16x32_bf16 a[20:23], v[144:147], a[236:239], a[20:23]\n" \
  "v_add_f32 v239, v239, v35\n" \
  "v_add_f32 v240, v240, v36\n" \
  "v_mfma_f32_16x16x32_bf16 a[24:27], v[148:151], a[236:239], a[24:27]\n" \
  "buffer_load_dwordx4 a[100:103], v16, s[36:39], s17 offen offset:1024 sc1\n" \
  "v_add_f32 v241, v241, v37\n" \
  "v_mfma_f32_16x16x32_bf16 a[28:31], v[152:155], a[236:239], a[28:31]\n" \
  "v_add_f32 v242, v242, v38\n" \
  "v_add_f32 v243, v243, v39\n" \
  "s_waitcnt vmcnt(10)\n" \
  "v_mfma_f32_16x16x32_bf16 a[44:47], v[152:155], a[240:243], a[44:47]\n" \
  "v_add_f32 v236, v236, v252\n" \
  "v_add_f32 v237, v237, v253\n" \
  "v_mfma_f32_16x16x32_bf16 a[40:43], v[148:151], a[240:243], a[40:43]\n" \
  "v_mfma_f32_16x16x32_bf16 a[36:39], v[144:147], a[240:243], a[36:39]\n" \
  "v_mfma_f32_16x16x32_bf16 a[32:35], a[232:235], a[240:243], a[32:35]\n" \
  "s_waitcnt vmcnt(9)\n" \
  "v_mfma_f32_16x16x32_bf16 a[16:19], v[156:159], a[160:163], a[16:19]\n" \
  "v_add_f32 v238, v238, v254\n" \
  "v_add_f32 v239, v239, v255\n" \
  "v_mfma_f32_16x16x32_bf16 a[20:23], a[224:227], a[160:163], a[20:23]\n" \
  "v_add_f32 v240, v240, v164\n" \
  "v_add_f32 v241, v241, v165\n" \
  "v_mfma_f32_16x16x32_bf16 a[24:27], a[228:231], a[160:163], a[24:27]\n" \
  "v_add_f32 v242, v242, v166\n" \
  "v_add_f32 v243, v243, v167\n" \
  "v_mfma_f32_16x16x32_bf16 a[28:31], a[48:51], a[160:163], a[28:31]\n" \
  "v_cvt_pk_bf16_f32 v168, v236, v237\n" \
  "v_cvt_pk_bf16_f32 v169, v238, v239\n" \
  "s_waitcnt vmcnt(8)\n" \
  "v_mfma_f32_16x16x32_bf16 a[44:47], a[48:51], a[252:255], a[44:47]\n" \
  "v_cvt_pk_bf16_f32 v170, v240, v241\n" \
  "v_mfma_f32_16x16x32_bf16 a[40:43], a[228:231], a[252:255], a[40:43]\n" \
  "v_mfma_f32_16x16x32_bf16 a[36:39], a[224:227], a[252:255], a[36:39]\n" \
  "v_mfma_f32_16x16x32_bf16 a[32:35], v[156:159], a[252:255], a[32:35]\n" \
  "s_waitcnt vmcnt(7)\n" \
  "v_mfma_f32_16x16x32_bf16 a[16:19], a[52:55], a[72:75], a[16:19]\n" \
  "v_cvt_pk_bf16_f32 v171, v242, v243\n" \
  "buffer_store_dwordx4 v[168:171], v28, s[44:47], 0 offen\n" \
  "v_mfma_f32_16x16x32_bf16 a[20:23], a[164:167], a[72:75], a[20:23]\n" \
  "v_mfma_f32_16x16x32_bf16 a[24:27], a[168:171], a[72:75], a[24:27]\n" \
  "v_mfma_f32_16x16x32_bf16 a[28:31], a[172:175], a[72:75], a[28:31]\n" \
  "s_waitcnt vmcnt(7)\n" \
  "v_mfma_f32_16x16x32_bf16 a[44:47], a[172:175], a[76:79], a[44:47]\n" \
  "v_mfma_f32_16x16x32_bf16 a[40:43], a[168:171], a[76:79], a[40:43]\n" \
  "v_mfma_f32_16x16x32_bf16 a[36:39], a[164:167], a[76:79], a[36:39]\n" \
  "v_mfma_f32_16x16x32_bf16 a[32:35], a[52:55], a[76:79], a[32:35]\n" \
  "s_waitcnt vmcnt(6)\n" \
  "v_mfma_f32_16x16x32_bf16 a[16:19], a[176:179], a[244:247], a[16:19]\n" \
  "v_mfma_f32_16x16x32_bf16 a[20:23], v[88:91], a[244:247], a[20:23]\n" \
  "v_mfma_f32_16x16x32_bf16 a[24:27], v[92:95], a[244:247], a[24:27]\n" \
  "v_mfma_f32_16x16x32_bf16 a[28:31], v[96:99], a[244:247], a[28:31]\n" \
  "s_waitcnt vmcnt(5)\n" \
  "v_mfma_f32_16x16x32_bf16 a[44:47], v[96:99], a[248:251], a[44:47]\n" \
  "v_mfma_f32_16x16x32_bf16 a[40:43], v[92:95], a[248:251], a[40:43]\n" \
  "v_mfma_f32_16x16x32_bf16 a[36:39], v[88:91], a[248:251], a[36:39]\n" \
  "v_mfma_f32_16x16x32_bf16 a[32:35], a[176:179], a[248:251], a[32:35]\n" \
  "s_waitcnt vmcnt(4)\n" \
  "v_mfma_f32_16x16x32_bf16 a[16:19], v[100:103], a[80:83], a[16:19]\n" \
  "v_accvgpr_read_b32 v33, a140\n" \
  "v_accvgpr_read_b32 v35, a141\n" \
  "v_mfma_f32_16x16x32_bf16 a[20:23], a[188:191], a[80:83], a[20:23]\n" \
  "v_accvgpr_read_b32 v37, a142\n" \
  "v_accvgpr_read_b32 v39, a143\n" \
  "v_mfma_f32_16x16x32_bf16 a[24:27], a[192:195], a[80:83], a[24:27]\n" \
  "v_lshlrev_b32 v32, 16, v33\n" \
  "v_lshlrev_b32 v34, 16, v35\n" \
  "v_mfma_f32_16x16x32_bf16 a[28:31], a[196:199], a[80:83], a[28:31]\n" \
  "v_lshlrev_b32 v36, 16, v37\n" \
  "v_lshlrev_b32 v38, 16, v39\n" \
  "s_waitcnt vmcnt(3)\n" \
  "v_mfma_f32_16x16x32_bf16 a[44:47], a[196:199], a[84:87], a[44:47]\n" \
  "v_and_b32 v33, 0xffff0000, v33\n" \
  "v_and_b32 v35, 0xffff0000, v35\n" \
  "v_mfma_f32_16x16x32_bf16 a[40:43], a[192:195], a[84:87], a[40:43]\n" \
  "v_and_b32 v37, 0xffff0000, v37\n" \
  "v_and_b32 v39, 0xffff0000, v39\n" \
  "v_mfma_f32_16x16x32_bf16 a[36:39], a[188:191], a[84:87], a[36:39]\n" \
  "v_mfma_f32_16x16x32_bf16 a[32:35], v[100:103], a[84:87], a[32:35]\n" \
  "s_waitcnt vmcnt(2)\n" \
  "v_mfma_f32_16x16x32_bf16 a[16:19], a[200:203], a[96:99], a[16:19]\n" \
  "v_mfma_f32_16x16x32_bf16 a[20:23], v[136:139], a[96:99], a[20:23]\n" \
  "v_mfma_f32_16x16x32_bf16 a[24:27], v[140:143], a[96:99], a[24:27]\n" \
  "v_mfma_f32_16x16x32_bf16 a[28:31], a[204:207], a[96:99], a[28:31]\n" \
  "s_waitcnt vmcnt(1)\n" \
  "v_mfma_f32_16x16x32_bf16 a[44:47], a[204:207], a[100:103], a[44:47]\n" \
  "v_mfma_f32_16x16x32_bf16 a[40:43], v[140:143], a[100:103], a[40:43]\n" \
  "v_mfma_f32_16x16x32_bf16 a[36:39], v[136:139], a[100:103], a[36:39]\n" \
  "v_mfma_f32_16x16x32_bf16 a[32:35], a[200:203], a[100:103], a[32:35]\n" \
  "ds_write_b128 v31, a[16:19]\n" \
  "ds_write_b128 v31, a[20:23] offset:1024\n" \
  "ds_write_b128 v31, a[24:27] offset:8192\n" \
  "ds_write_b128 v31, a[28:31] offset:9216\n" \
  "s_nop 3\n" \
  "ds_write_b128 v31, a[32:35] offset:16384\n" \
  "ds_write_b128 v31, a[36:39] offset:17408\n" \
  "ds_write_b128 v31, a[40:43] offset:24576\n" \
  "ds_write_b128 v31, a[44:47] offset:25600\n" \
  "s_waitcnt lgkmcnt(0)\n" \
  "s_barrier\n" \
  "ds_read_b128 v[236:239], v9\n" \
  "ds_read_b128 v[240:243], v9 offset:1024\n" \
  "ds_read_b128 v[244:247], v9 offset:2048\n" \
  "ds_read_b128 v[248:251], v9 offset:3072\n" \
  "ds_read_b128 v[252:255], v9 offset:4096\n" \
  "ds_read_b128 v[164:167], v9 offset:5120\n" \
  "ds_read_b128 v[168:171], v9 offset:6144\n" \
  "ds_read_b128 v[172:175], v9 offset:7168\n" \
  "s_waitcnt lgkmcnt(0)\n" \
  "v_pk_add_f32 v[236:237], v[236:237], v[244:245]\n" \
  "v_pk_add_f32 v[238:239], v[238:239], v[246:247]\n" \
  "v_pk_add_f32 v[252:253], v[252:253], v[168:169]\n" \
  "v_pk_add_f32 v[254:255], v[254:255], v[170:171]\n" \
  "v_pk_add_f32 v[240:241], v[240:241], v[248:249]\n" \
  "v_pk_add_f32 v[242:243], v[242:243], v[250:251]\n" \
  "v_pk_add_f32 v[164:165], v[164:165], v[172:173]\n" \
  "v_pk_add_f32 v[166:167], v[166:167], v[174:175]\n" \
  "v_pk_add_f32 v[236:237], v[236:237], v[32:33]\n" \
  "v_pk_add_f32 v[238:239], v[238:239], v[34:35]\n" \
  "v_pk_add_f32 v[240:241], v[240:241], v[36:37]\n" \
  "v_pk_add_f32 v[242:243], v[242:243], v[38:39]\n" \
  "v_pk_add_f32 v[236:237], v[236:237], v[252:253]\n" \
  "v_pk_add_f32 v[238:239], v[238:239], v[254:255]\n" \
  "v_pk_add_f32 v[240:241], v[240:241], v[164:165]\n" \
  "v_pk_add_f32 v[242:243], v[242:243], v[166:167]\n" \
  "v_cvt_pk_bf16_f32 v168, v236, v237\n" \
  "v_cvt_pk_bf16_f32 v169, v238, v239\n" \
  "v_cvt_pk_bf16_f32 v170, v240, v241\n" \
  "v_cvt_pk_bf16_f32 v171, v242, v243\n" \
  "buffer_store_dwordx4 v[168:171], v29, s[44:47], 0 offen\n" \
  "s_branch Lend%=\n" \
  "LX_slow%=:\n" \
  "s_movk_i32 s69, 0x7fff\n" \
  "LX_loop%=:\n" \
  "s_not_b64 exec, vcc\n" \
  "buffer_load_dwordx4 v[244:247], v96, s[36:39], s20 offen sc1\n" \
  "buffer_load_dwordx4 v[248:251], v96, s[36:39], s21 offen sc1\n" \
  "buffer_load_dwordx4 v[252:255], v96, s[36:39], s22 offen sc1\n" \
  "buffer_load_dwordx4 v[104:107], v96, s[36:39], s23 offen sc1\n" \
  "buffer_load_dwordx4 v[108:111], v96, s[36:39], s24 offen sc1\n" \
  "buffer_load_dwordx4 v[112:115], v96, s[36:39], s25 offen sc1\n" \
  "buffer_load_dwordx4 v[64:67], v96, s[36:39], s26 offen sc1\n" \
  "buffer_load_dwordx4 v[32:35], v96, s[36:39], s27 offen sc1\n" \
  "s_mov_b64 exec, -1\n" \
  "s_waitcnt vmcnt(0)\n" \
  "v_xor_b32 v2, s48, v247\n" \
  "v_xor_b32 v3, s67, v246\n" \
  "v_bitop3_b32 v2, v2, v251, s48 bitop3:0xf6\n" \
  "v_bitop3_b32 v3, v3, v250, s67 bitop3:0xf6\n" \
  "v_bitop3_b32 v2, v2, v255, s48 bitop3:0xf6\n" \
  "v_bitop3_b32 v3, v3, v254, s67 bitop3:0xf6\n" \
  "v_bitop3_b32 v2, v2, v107, s48 bitop3:0xf6\n" \
  "v_bitop3_b32 v3, v3, v106, s67 bitop3:0xf6\n" \
  "v_bitop3_b32 v2, v2, v111, s48 bitop3:0xf6\n" \
  "v_bitop3_b32 v3, v3, v110, s67 bitop3:0xf6\n" \
  "v_bitop3_b32 v2, v2, v115, s48 bitop3:0xf6\n" \
  "v_bitop3_b32 v3, v3, v114, s67 bitop3:0xf6\n" \
  "v_bitop3_b32 v2, v2, v67, s48 bitop3:0xf6\n" \
  "v_bitop3_b32 v3, v3, v66, s67 bitop3:0xf6\n" \
  "v_bitop3_b32 v2, v2, v35, s48 bitop3:0xf6\n" \
  "v_bitop3_b32 v3, v3, v34, s67 bitop3:0xf6\n" \
  "v_lshrrev_b32 v3, 16, v3\n" \
  "v_or_b32 v2, v2, v3\n" \
  "v_cmp_eq_u32 vcc, 0, v2\n" \
  "s_nop 1\n" \
  "s_cmp_eq_u64 vcc, -1\n" \
  "s_cbranch_scc1 LX_ok%=\n" \
  "s_sub_u32 s69, s69, 1\n" \
  "s_cbranch_scc0 LX_loop%=\n" \
  "s_branch LX_ok%=\n" \
  "LW_slow%=:\n" \
  "s_movk_i32 s69, 0x7fff\n" \
  "LW_loop%=:\n" \
  "s_not_b64 exec, vcc\n" \
  "buffer_load_dwordx4 v[244:247], v96, s[36:39], s20 offen sc1\n" \
  "buffer_load_dwordx4 v[248:251], v96, s[36:39], s21 offen sc1\n" \
  "buffer_load_dwordx4 v[252:255], v96, s[36:39], s22 offen sc1\n" \
  "buffer_load_dwordx4 v[104:107], v96, s[36:39], s23 offen sc1\n" \
  "buffer_load_dwordx4 v[108:111], v96, s[36:39], s24 offen sc1\n" \
  "buffer_load_dwordx4 v[112:115], v96, s[36:39], s25 offen sc1\n" \
  "buffer_load_dwordx4 v[64:67], v96, s[36:39], s26 offen sc1\n" \
  "buffer_load_dwordx4 v[32:35], v96, s[36:39], s27 offen sc1\n" \
  "s_mov_b64 exec, -1\n" \
  "s_waitcnt vmcnt(0)\n" \
  "v_xor_b32 v2, s48, v247\n" \
  "v_xor_b32 v3, s67, v246\n" \
  "v_bitop3_b32 v2, v2, v251, s48 bitop3:0xf6\n" \
  "v_bitop3_b32 v3, v3, v250, s67 bitop3:0xf6\n" \
  "v_bitop3_b32 v2, v2, v255, s48 bitop3:0xf6\n" \
  "v_bitop3_b32 v3, v3, v254, s67 bitop3:0xf6\n" \
  "v_bitop3_b32 v2, v2, v107, s48 bitop3:0xf6\n" \
  "v_bitop3_b32 v3, v3, v106, s67 bitop3:0xf6\n" \
  "v_bitop3_b32 v2, v2, v111, s48 bitop3:0xf6\n" \
  "v_bitop3_b32 v3, v3, v110, s67 bitop3:0xf6\n" \
  "v_bitop3_b32 v2, v2, v115, s48 bitop3:0xf6\n" \
  "v_bitop3_b32 v3, v3, v114, s67 bitop3:0xf6\n" \
  "v_bitop3_b32 v2, v2, v67, s48 bitop3:0xf6\n" \
  "v_bitop3_b32 v3, v3, v66, s67 bitop3:0xf6\n" \
  "v_bitop3_b32 v2, v2, v35, s48 bitop3:0xf6\n" \
  "v_bitop3_b32 v3, v3, v34, s67 bitop3:0xf6\n" \
  "v_lshrrev_b32 v3, 16, v3\n" \
  "v_or_b32 v2, v2, v3\n" \
  "v_cmp_eq_u32 vcc, 0, v2\n" \
  "s_nop 1\n" \
  "s_cmp_eq_u64 vcc, -1\n" \
  "s_cbranch_scc1 LW_ok%=\n" \
  "s_sub_u32 s69, s69, 1\n" \
  "s_cbranch_scc0 LW_loop%=\n" \
  "s_branch LW_ok%=\n" \
  "LF0_slow%=:\n" \
  "s_movk_i32 s69, 0x7fff\n" \
  "LF0_loop%=:\n" \
  "s_not_b64 exec, vcc\n" \
  "buffer_load_dwordx2 v[26:27], v160, s[36:39], 0 offen sc1\n" \
  "s_mov_b64 exec, -1\n" \
  "s_waitcnt vmcnt(0)\n" \
  "v_cmp_eq_u64 vcc, s[48:49], v[26:27]\n" \
  "s_nop 1\n" \
  "s_cmp_eq_u64 vcc, -1\n" \
  "s_cbranch_scc1 LF0_ok%=\n" \
  "s_sub_u32 s69, s69, 1\n" \
  "s_cbranch_scc0 LF0_loop%=\n" \
  "s_branch LF0_ok%=\n" \
  "LS0_slow%=:\n" \
  "s_movk_i32 s69, 0x7fff\n" \
  "LS0_loop%=:\n" \
  "s_not_b64 exec, vcc\n" \
  "s_mov_b32 s12, 0x800000\n" \
  "buffer_load_dwordx4 a[56:59], v14, s[36:39], s12 offen sc1\n" \
  "buffer_load_dwordx4 a[60:63], v15, s[36:39], s12 offen sc1\n" \
  "buffer_load_dwordx4 a[64:67], v16, s[36:39], s12 offen sc1\n" \
  "buffer_load_dwordx4 a[68:71], v17, s[36:39], s12 offen sc1\n" \
  "s_mov_b64 exec, -1\n" \
  "s_waitcnt vmcnt(0)\n" \
  "v_accvgpr_read_b32 v164, a56\n" \
  "v_accvgpr_read_b32 v168, a57\n" \
  "v_accvgpr_read_b32 v172, a58\n" \
  "v_accvgpr_read_b32 v176, a59\n" \
  "v_accvgpr_read_b32 v165, a60\n" \
  "v_accvgpr_read_b32 v169, a61\n" \
  "v_accvgpr_read_b32 v173, a62\n" \
  "v_accvgpr_read_b32 v177, a63\n" \
  "v_accvgpr_read_b32 v166, a64\n" \
  "v_accvgpr_read_b32 v170, a65\n" \
  "v_accvgpr_read_b32 v174, a66\n" \
  "v_accvgpr_read_b32 v178, a67\n" \
  "v_accvgpr_read_b32 v167, a68\n" \
  "v_accvgpr_read_b32 v171, a69\n" \
  "v_accvgpr_read_b32 v175, a70\n" \
  "v_accvgpr_read_b32 v179, a71\n" \
  "s_waitcnt vmcnt(0)\n" \
  "v_max3_u32 v2, v164, v165, v166\n" \
  "v_max_u32 v2, v2, v167\n" \
  "v_cmp_ne_u32 vcc, -1, v2\n" \
  "s_nop 1\n" \
  "s_cmp_eq_u64 vcc, -1\n" \
  "s_cbranch_scc1 LS0_ok%=\n" \
  "s_sub_u32 s69, s69, 1\n" \
  "s_cbranch_scc0 LS0_loop%=\n" \
  "s_branch LS0_ok%=\n" \
  "LS1_slow%=:\n" \
  "s_movk_i32 s69, 0x7fff\n" \
  "LS1_loop%=:\n" \
  "s_not_b64 exec, vcc\n" \
  "s_mov_b32 s12, 0x808000\n" \
  "buffer_load_dwordx4 a[72:75], v14, s[36:39], s12 offen sc1\n" \
  "buffer_load_dwordx4 a[76:79], v15, s[36:39], s12 offen sc1\n" \
  "buffer_load_dwordx4 a[80:83], v16, s[36:39], s12 offen sc1\n" \
  "buffer_load_dwordx4 a[84:87], v17, s[36:39], s12 offen sc1\n" \
  "s_mov_b64 exec, -1\n" \
  "s_waitcnt vmcnt(0)\n" \
  "v_accvgpr_read_b32 v180, a72\n" \
  "v_accvgpr_read_b32 v184, a73\n" \
  "v_accvgpr_read_b32 v188, a74\n" \
  "v_accvgpr_read_b32 v192, a75\n" \
  "v_accvgpr_read_b32 v181, a76\n" \
  "v_accvgpr_read_b32 v185, a77\n" \
  "v_accvgpr_read_b32 v189, a78\n" \
  "v_accvgpr_read_b32 v193, a79\n" \
  "v_accvgpr_read_b32 v182, a80\n" \
  "v_accvgpr_read_b32 v186, a81\n" \
  "v_accvgpr_read_b32 v190, a82\n" \
  "v_accvgpr_read_b32 v194, a83\n" \
  "v_accvgpr_read_b32 v183, a84\n" \
  "v_accvgpr_read_b32 v187, a85\n" \
  "v_accvgpr_read_b32 v191, a86\n" \
  "v_accvgpr_read_b32 v195, a87\n" \
  "s_waitcnt vmcnt(0)\n" \
  "v_max3_u32 v2, v180, v181, v182\n" \
  "v_max_u32 v2, v2, v183\n" \
  "v_cmp_ne_u32 vcc, -1, v2\n" \
  "s_nop 1\n" \
  "s_cmp_eq_u64 vcc, -1\n" \
  "s_cbranch_scc1 LS1_ok%=\n" \
  "s_sub_u32 s69, s69, 1\n" \
  "s_cbranch_scc0 LS1_loop%=\n" \
  "s_branch LS1_ok%=\n" \
  "LS2_slow%=:\n" \
  "s_movk_i32 s69, 0x7fff\n" \
  "LS2_loop%=:\n" \
  "s_not_b64 exec, vcc\n" \
  "s_mov_b32 s12, 0x810000\n" \
  "buffer_load_dwordx4 a[88:91], v14, s[36:39], s12 offen sc1\n" \
  "buffer_load_dwordx4 a[92:95], v15, s[36:39], s12 offen sc1\n" \
  "buffer_load_dwordx4 a[96:99], v16, s[36:39], s12 offen sc1\n" \
  "buffer_load_dwordx4 a[100:103], v17, s[36:39], s12 offen sc1\n" \
  "s_mov_b64 exec, -1\n" \
  "s_waitcnt vmcnt(0)\n" \
  "v_accvgpr_read_b32 v164, a88\n" \
  "v_accvgpr_read_b32 v168, a89\n" \
  "v_accvgpr_read_b32 v172, a90\n" \
  "v_accvgpr_read_b32 v176, a91\n" \
  "v_accvgpr_read_b32 v165, a92\n" \
  "v_accvgpr_read_b32 v169, a93\n" \
  "v_accvgpr_read_b32 v173, a94\n" \
  "v_accvgpr_read_b32 v177, a95\n" \
  "v_accvgpr_read_b32 v166, a96\n" \
  "v_accvgpr_read_b32 v170, a97\n" \
  "v_accvgpr_read_b32 v174, a98\n" \
  "v_accvgpr_read_b32 v178, a99\n" \
  "v_accvgpr_read_b32 v167, a100\n" \
  "v_accvgpr_read_b32 v171, a101\n" \
  "v_accvgpr_read_b32 v175, a102\n" \
  "v_accvgpr_read_b32 v179, a103\n" \
  "s_waitcnt vmcnt(0)\n" \
  "v_max3_u32 v2, v164, v165, v166\n" \
  "v_max_u32 v2, v2, v167\n" \
  "v_cmp_ne_u32 vcc, -1, v2\n" \
  "s_nop 1\n" \
  "s_cmp_eq_u64 vcc, -1\n" \
  "s_cbranch_scc1 LS2_ok%=\n" \
  "s_sub_u32 s69, s69, 1\n" \
  "s_cbranch_scc0 LS2_loop%=\n" \
  "s_branch LS2_ok%=\n" \
  "LS3_slow%=:\n" \
  "s_movk_i32 s69, 0x7fff\n" \
  "LS3_loop%=:\n" \
  "s_not_b64 exec, vcc\n" \
  "s_mov_b32 s12, 0x818000\n" \
  "buffer_load_dwordx4 a[104:107], v14, s[36:39], s12 offen sc1\n" \
  "buffer_load_dwordx4 a[108:111], v15, s[36:39], s12 offen sc1\n" \
  "buffer_load_dwordx4 a[112:115], v16, s[36:39], s12 offen sc1\n" \
  "buffer_load_dwordx4 a[116:119], v17, s[36:39], s12 offen sc1\n" \
  "s_mov_b64 exec, -1\n" \
  "s_waitcnt vmcnt(0)\n" \
  "v_accvgpr_read_b32 v180, a104\n" \
  "v_accvgpr_read_b32 v184, a105\n" \
  "v_accvgpr_read_b32 v188, a106\n" \
  "v_accvgpr_read_b32 v192, a107\n" \
  "v_accvgpr_read_b32 v181, a108\n" \
  "v_accvgpr_read_b32 v185, a109\n" \
  "v_accvgpr_read_b32 v189, a110\n" \
  "v_accvgpr_read_b32 v193, a111\n" \
  "v_accvgpr_read_b32 v182, a112\n" \
  "v_accvgpr_read_b32 v186, a113\n" \
  "v_accvgpr_read_b32 v190, a114\n" \
  "v_accvgpr_read_b32 v194, a115\n" \
  "v_accvgpr_read_b32 v183, a116\n" \
  "v_accvgpr_read_b32 v187, a117\n" \
  "v_accvgpr_read_b32 v191, a118\n" \
  "v_accvgpr_read_b32 v195, a119\n" \
  "s_waitcnt vmcnt(0)\n" \
  "v_max3_u32 v2, v180, v181, v182\n" \
  "v_max_u32 v2, v2, v183\n" \
  "v_cmp_ne_u32 vcc, -1, v2\n" \
  "s_nop 1\n" \
  "s_cmp_eq_u64 vcc, -1\n" \
  "s_cbranch_scc1 LS3_ok%=\n" \
  "s_sub_u32 s69, s69, 1\n" \
  "s_cbranch_scc0 LS3_loop%=\n" \
  "s_branch LS3_ok%=\n" \
  "LS4_slow%=:\n" \
  "s_movk_i32 s69, 0x7fff\n" \
  "LS4_loop%=:\n" \
  "s_not_b64 exec, vcc\n" \
  "s_mov_b32 s12, 0x820000\n" \
  "buffer_load_dwordx4 a[120:123], v14, s[36:39], s12 offen sc1\n" \
  "buffer_load_dwordx4 a[124:127], v15, s[36:39], s12 offen sc1\n" \
  "buffer_load_dwordx4 a[128:131], v16, s[36:39], s12 offen sc1\n" \
  "buffer_load_dwordx4 a[132:135], v17, s[36:39], s12 offen sc1\n" \
  "s_mov_b64 exec, -1\n" \
  "s_waitcnt vmcnt(0)\n" \
  "v_accvgpr_read_b32 v164, a120\n" \
  "v_accvgpr_read_b32 v168, a121\n" \
  "v_accvgpr_read_b32 v172, a122\n" \
  "v_accvgpr_read_b32 v176, a123\n" \
  "v_accvgpr_read_b32 v165, a124\n" \
  "v_accvgpr_read_b32 v169, a125\n" \
  "v_accvgpr_read_b32 v173, a126\n" \
  "v_accvgpr_read_b32 v177, a127\n" \
  "v_accvgpr_read_b32 v166, a128\n" \
  "v_accvgpr_read_b32 v170, a129\n" \
  "v_accvgpr_read_b32 v174, a130\n" \
  "v_accvgpr_read_b32 v178, a131\n" \
  "v_accvgpr_read_b32 v167, a132\n" \
  "v_accvgpr_read_b32 v171, a133\n" \
  "v_accvgpr_read_b32 v175, a134\n" \
  "v_accvgpr_read_b32 v179, a135\n" \
  "s_waitcnt vmcnt(0)\n" \
  "v_max3_u32 v2, v164, v165, v166\n" \
  "v_max_u32 v2, v2, v167\n" \
  "v_cmp_ne_u32 vcc, -1, v2\n" \
  "s_nop 1\n" \
  "s_cmp_eq_u64 vcc, -1\n" \
  "s_cbranch_scc1 LS4_ok%=\n" \
  "s_sub_u32 s69, s69, 1\n" \
  "s_cbranch_scc0 LS4_loop%=\n" \
  "s_branch LS4_ok%=\n" \
  "LS5_slow%=:\n" \
  "s_movk_i32 s69, 0x7fff\n" \
  "LS5_loop%=:\n" \
  "s_not_b64 exec, vcc\n" \
  "s_mov_b32 s12, 0x828000\n" \
  "buffer_load_dwordx4 a[56:59], v14, s[36:39], s12 offen sc1\n" \
  "buffer_load_dwordx4 a[60:63], v15, s[36:39], s12 offen sc1\n" \
  "buffer_load_dwordx4 a[64:67], v16, s[36:39], s12 offen sc1\n" \
  "buffer_load_dwordx4 a[68:71], v17, s[36:39], s12 offen sc1\n" \
  "s_mov_b64 exec, -1\n" \
  "s_waitcnt vmcnt(0)\n" \
  "v_accvgpr_read_b32 v180, a56\n" \
  "v_accvgpr_read_b32 v184, a57\n" \
  "v_accvgpr_read_b32 v188, a58\n" \
  "v_accvgpr_read_b32 v192, a59\n" \
  "v_accvgpr_read_b32 v181, a60\n" \
  "v_accvgpr_read_b32 v185, a61\n" \
  "v_accvgpr_read_b32 v189, a62\n" \
  "v_accvgpr_read_b32 v193, a63\n" \
  "v_accvgpr_read_b32 v182, a64\n" \
  "v_accvgpr_read_b32 v186, a65\n" \
  "v_accvgpr_read_b32 v190, a66\n" \
  "v_accvgpr_read_b32 v194, a67\n" \
  "v_accvgpr_read_b32 v183, a68\n" \
  "v_accvgpr_read_b32 v187, a69\n" \
  "v_accvgpr_read_b32 v191, a70\n" \
  "v_accvgpr_read_b32 v195, a71\n" \
  "s_waitcnt vmcnt(0)\n" \
  "v_max3_u32 v2, v180, v181, v182\n" \
  "v_max_u32 v2, v2, v183\n" \
  "v_cmp_ne_u32 vcc, -1, v2\n" \
  "s_nop 1\n" \
  "s_cmp_eq_u64 vcc, -1\n" \
  "s_cbranch_scc1 LS5_ok%=\n" \
  "s_sub_u32 s69, s69, 1\n" \
  "s_cbranch_scc0 LS5_loop%=\n" \
  "s_branch LS5_ok%=\n" \
  "LS6_slow%=:\n" \
  "s_movk_i32 s69, 0x7fff\n" \
  "LS6_loop%=:\n" \
  "s_not_b64 exec, vcc\n" \
  "s_mov_b32 s12, 0x830000\n" \
  "buffer_load_dwordx4 a[72:75], v14, s[36:39], s12 offen sc1\n" \
  "buffer_load_dwordx4 a[76:79], v15, s[36:39], s12 offen sc1\n" \
  "buffer_load_dwordx4 a[80:83], v16, s[36:39], s12 offen sc1\n" \
  "buffer_load_dwordx4 a[84:87], v17, s[36:39], s12 offen sc1\n" \
  "s_mov_b64 exec, -1\n" \
  "s_waitcnt vmcnt(0)\n" \
  "v_accvgpr_read_b32 v164, a72\n" \
  "v_accvgpr_read_b32 v168, a73\n" \
  "v_accvgpr_read_b32 v172, a74\n" \
  "v_accvgpr_read_b32 v176, a75\n" \
  "v_accvgpr_read_b32 v165, a76\n" \
  "v_accvgpr_read_b32 v169, a77\n" \
  "v_accvgpr_read_b32 v173, a78\n" \
  "v_accvgpr_read_b32 v177, a79\n" \
  "v_accvgpr_read_b32 v166, a80\n" \
  "v_accvgpr_read_b32 v170, a81\n" \
  "v_accvgpr_read_b32 v174, a82\n" \
  "v_accvgpr_read_b32 v178, a83\n" \
  "v_accvgpr_read_b32 v167, a84\n" \
  "v_accvgpr_read_b32 v171, a85\n" \
  "v_accvgpr_read_b32 v175, a86\n" \
  "v_accvgpr_read_b32 v179, a87\n" \
  "s_waitcnt vmcnt(0)\n" \
  "v_max3_u32 v2, v164, v165, v166\n" \
  "v_max_u32 v2, v2, v167\n" \
  "v_cmp_ne_u32 vcc, -1, v2\n" \
  "s_nop 1\n" \
  "s_cmp_eq_u64 vcc, -1\n" \
  "s_cbranch_scc1 LS6_ok%=\n" \
  "s_sub_u32 s69, s69, 1\n" \
  "s_cbranch_scc0 LS6_loop%=\n" \
  "s_branch LS6_ok%=\n" \
  "LFD_slow%=:\n" \
  "s_movk_i32 s69, 0x7fff\n" \
  "LFD_loop%=:\n" \
  "s_not_b64 exec, vcc\n" \
  "buffer_load_dwordx2 v[26:27], v6, s[36:39], 0 offen sc1\n" \
  "s_mov_b64 exec, -1\n" \
  "s_waitcnt vmcnt(0)\n" \
  "v_cmp_eq_u64 vcc, s[48:49], v[26:27]\n" \
  "s_nop 1\n" \
  "s_cmp_eq_u64 vcc, -1\n" \
  "s_cbranch_scc1 LFD_ok%=\n" \
  "s_sub_u32 s69, s69, 1\n" \
  "s_cbranch_scc0 LFD_loop%=\n" \
  "s_branch LFD_ok%=\n" \
  "LS7_slow%=:\n" \
  "s_movk_i32 s69, 0x7fff\n" \
  "LS7_loop%=:\n" \
  "s_not_b64 exec, vcc\n" \
  "s_mov_b32 s12, 0x838000\n" \
  "buffer_load_dwordx4 a[88:91], v14, s[36:39], s12 offen sc1\n" \
  "buffer_load_dwordx4 a[92:95], v15, s[36:39], s12 offen sc1\n" \
  "buffer_load_dwordx4 a[96:99], v16, s[36:39], s12 offen sc1\n" \
  "buffer_load_dwordx4 a[100:103], v17, s[36:39], s12 offen sc1\n" \
  "s_mov_b64 exec, -1\n" \
  "s_waitcnt vmcnt(0)\n" \
  "v_accvgpr_read_b32 v180, a88\n" \
  "v_accvgpr_read_b32 v184, a89\n" \
  "v_accvgpr_read_b32 v188, a90\n" \
  "v_accvgpr_read_b32 v192, a91\n" \
  "v_accvgpr_read_b32 v181, a92\n" \
  "v_accvgpr_read_b32 v185, a93\n" \
  "v_accvgpr_read_b32 v189, a94\n" \
  "v_accvgpr_read_b32 v193, a95\n" \
  "v_accvgpr_read_b32 v182, a96\n" \
  "v_accvgpr_read_b32 v186, a97\n" \
  "v_accvgpr_read_b32 v190, a98\n" \
  "v_accvgpr_read_b32 v194, a99\n" \
  "v_accvgpr_read_b32 v183, a100\n" \
  "v_accvgpr_read_b32 v187, a101\n" \
  "v_accvgpr_read_b32 v191, a102\n" \
  "v_accvgpr_read_b32 v195, a103\n" \
  "s_waitcnt vmcnt(0)\n" \
  "v_max3_u32 v2, v180, v181, v182\n" \
  "v_max_u32 v2, v2, v183\n" \
  "v_cmp_ne_u32 vcc, -1, v2\n" \
  "s_nop 1\n" \
  "s_cmp_eq_u64 vcc, -1\n" \
  "s_cbranch_scc1 LS7_ok%=\n" \
  "s_sub_u32 s69, s69, 1\n" \
  "s_cbranch_scc0 LS7_loop%=\n" \
  "s_branch LS7_ok%=\n" \
  "LS8_slow%=:\n" \
  "s_movk_i32 s69, 0x7fff\n" \
  "LS8_loop%=:\n" \
  "s_not_b64 exec, vcc\n" \
  "s_mov_b32 s12, 0x900000\n" \
  "buffer_load_dwordx4 a[104:107], v14, s[36:39], s12 offen sc1\n" \
  "buffer_load_dwordx4 a[108:111], v15, s[36:39], s12 offen sc1\n" \
  "buffer_load_dwordx4 a[112:115], v16, s[36:39], s12 offen sc1\n" \
  "buffer_load_dwordx4 a[116:119], v17, s[36:39], s12 offen sc1\n" \
  "s_mov_b64 exec, -1\n" \
  "s_waitcnt vmcnt(0)\n" \
  "v_accvgpr_read_b32 v164, a104\n" \
  "v_accvgpr_read_b32 v168, a105\n" \
  "v_accvgpr_read_b32 v172, a106\n" \
  "v_accvgpr_read_b32 v176, a107\n" \
  "v_accvgpr_read_b32 v165, a108\n" \
  "v_accvgpr_read_b32 v169, a109\n" \
  "v_accvgpr_read_b32 v173, a110\n" \
  "v_accvgpr_read_b32 v177, a111\n" \
  "v_accvgpr_read_b32 v166, a112\n" \
  "v_accvgpr_read_b32 v170, a113\n" \
  "v_accvgpr_read_b32 v174, a114\n" \
  "v_accvgpr_read_b32 v178, a115\n" \
  "v_accvgpr_read_b32 v167, a116\n" \
  "v_accvgpr_read_b32 v171, a117\n" \
  "v_accvgpr_read_b32 v175, a118\n" \
  "v_accvgpr_read_b32 v179, a119\n" \
  "s_waitcnt vmcnt(0)\n" \
  "v_max3_u32 v2, v164, v165, v166\n" \
  "v_max_u32 v2, v2, v167\n" \
  "v_cmp_ne_u32 vcc, -1, v2\n" \
  "s_nop 1\n" \
  "s_cmp_eq_u64 vcc, -1\n" \
  "s_cbranch_scc1 LS8_ok%=\n" \
  "s_sub_u32 s69, s69, 1\n" \
  "s_cbranch_scc0 LS8_loop%=\n" \
  "s_branch LS8_ok%=\n" \
  "LS9_slow%=:\n" \
  "s_movk_i32 s69, 0x7fff\n" \
  "LS9_loop%=:\n" \
  "s_not_b64 exec, vcc\n" \
  "s_mov_b32 s12, 0x908000\n" \
  "buffer_load_dwordx4 a[120:123], v14, s[36:39], s12 offen sc1\n" \
  "buffer_load_dwordx4 a[124:127], v15, s[36:39], s12 offen sc1\n" \
  "buffer_load_dwordx4 a[128:131], v16, s[36:39], s12 offen sc1\n" \
  "buffer_load_dwordx4 a[132:135], v17, s[36:39], s12 offen sc1\n" \
  "s_mov_b64 exec, -1\n" \
  "s_waitcnt vmcnt(0)\n" \
  "v_accvgpr_read_b32 v180, a120\n" \
  "v_accvgpr_read_b32 v184, a121\n" \
  "v_accvgpr_read_b32 v188, a122\n" \
  "v_accvgpr_read_b32 v192, a123\n" \
  "v_accvgpr_read_b32 v181, a124\n" \
  "v_accvgpr_read_b32 v185, a125\n" \
  "v_accvgpr_read_b32 v189, a126\n" \
  "v_accvgpr_read_b32 v193, a127\n" \
  "v_accvgpr_read_b32 v182, a128\n" \
  "v_accvgpr_read_b32 v186, a129\n" \
  "v_accvgpr_read_b32 v190, a130\n" \
  "v_accvgpr_read_b32 v194, a131\n" \
  "v_accvgpr_read_b32 v183, a132\n" \
  "v_accvgpr_read_b32 v187, a133\n" \
  "v_accvgpr_read_b32 v191, a134\n" \
  "v_accvgpr_read_b32 v195, a135\n" \
  "s_waitcnt vmcnt(0)\n" \
  "v_max3_u32 v2, v180, v181, v182\n" \
  "v_max_u32 v2, v2, v183\n" \
  "v_cmp_ne_u32 vcc, -1, v2\n" \
  "s_nop 1\n" \
  "s_cmp_eq_u64 vcc, -1\n" \
  "s_cbranch_scc1 LS9_ok%=\n" \
  "s_sub_u32 s69, s69, 1\n" \
  "s_cbranch_scc0 LS9_loop%=\n" \
  "s_branch LS9_ok%=\n" \
  "LF2_slow%=:\n" \
  "s_movk_i32 s69, 0x7fff\n" \
  "LF2_loop%=:\n" \
  "s_not_b64 exec, vcc\n" \
  "buffer_load_dwordx2 v[26:27], v8, s[36:39], 0 offen sc1\n" \
  "s_mov_b64 exec, -1\n" \
  "s_waitcnt vmcnt(0)\n" \
  "v_cmp_eq_u64 vcc, s[48:49], v[26:27]\n" \
  "s_nop 1\n" \
  "s_cmp_eq_u64 vcc, -1\n" \
  "s_cbranch_scc1 LF2_ok%=\n" \
  "s_sub_u32 s69, s69, 1\n" \
  "s_cbranch_scc0 LF2_loop%=\n" \
  "s_branch LF2_ok%=\n" \
  "LS10_slow%=:\n" \
  "s_movk_i32 s69, 0x7fff\n" \
  "LS10_loop%=:\n" \
  "s_not_b64 exec, vcc\n" \
  "s_mov_b32 s12, 0x910000\n" \
  "buffer_load_dwordx4 a[56:59], v14, s[36:39], s12 offen sc1\n" \
  "buffer_load_dwordx4 a[60:63], v15, s[36:39], s12 offen sc1\n" \
  "buffer_load_dwordx4 a[64:67], v16, s[36:39], s12 offen sc1\n" \
  "buffer_load_dwordx4 a[68:71], v17, s[36:39], s12 offen sc1\n" \
  "s_mov_b64 exec, -1\n" \
  "s_waitcnt vmcnt(0)\n" \
  "v_accvgpr_read_b32 v164, a56\n" \
  "v_accvgpr_read_b32 v168, a57\n" \
  "v_accvgpr_read_b32 v172, a58\n" \
  "v_accvgpr_read_b32 v176, a59\n" \
  "v_accvgpr_read_b32 v165, a60\n" \
  "v_accvgpr_read_b32 v169, a61\n" \
  "v_accvgpr_read_b32 v173, a62\n" \
  "v_accvgpr_read_b32 v177, a63\n" \
  "v_accvgpr_read_b32 v166, a64\n" \
  "v_accvgpr_read_b32 v170, a65\n" \
  "v_accvgpr_read_b32 v174, a66\n" \
  "v_accvgpr_read_b32 v178, a67\n" \
  "v_accvgpr_read_b32 v167, a68\n" \
  "v_accvgpr_read_b32 v171, a69\n" \
  "v_accvgpr_read_b32 v175, a70\n" \
  "v_accvgpr_read_b32 v179, a71\n" \
  "s_waitcnt vmcnt(0)\n" \
  "v_max3_u32 v2, v164, v165, v166\n" \
  "v_max_u32 v2, v2, v167\n" \
  "v_cmp_ne_u32 vcc, -1, v2\n" \
  "s_nop 1\n" \
  "s_cmp_eq_u64 vcc, -1\n" \
  "s_cbranch_scc1 LS10_ok%=\n" \
  "s_sub_u32 s69, s69, 1\n" \
  "s_cbranch_scc0 LS10_loop%=\n" \
  "s_branch LS10_ok%=\n" \
  "LS11_slow%=:\n" \
  "s_movk_i32 s69, 0x7fff\n" \
  "LS11_loop%=:\n" \
  "s_not_b64 exec, vcc\n" \
  "s_mov_b32 s12, 0x918000\n" \
  "buffer_load_dwordx4 a[72:75], v14, s[36:39], s12 offen sc1\n" \
  "buffer_load_dwordx4 a[76:79], v15, s[36:39], s12 offen sc1\n" \
  "buffer_load_dwordx4 a[80:83], v16, s[36:39], s12 offen sc1\n" \
  "buffer_load_dwordx4 a[84:87], v17, s[36:39], s12 offen sc1\n" \
  "s_mov_b64 exec, -1\n" \
  "s_waitcnt vmcnt(0)\n" \
  "v_accvgpr_read_b32 v180, a72\n" \
  "v_accvgpr_read_b32 v184, a73\n" \
  "v_accvgpr_mov_b32 a180, a74\n" \
  "v_accvgpr_mov_b32 a184, a75\n" \
  "v_accvgpr_read_b32 v181, a76\n" \
  "v_accvgpr_read_b32 v185, a77\n" \
  "v_accvgpr_mov_b32 a181, a78\n" \
  "v_accvgpr_mov_b32 a185, a79\n" \
  "v_accvgpr_read_b32 v182, a80\n" \
  "v_accvgpr_read_b32 v186, a81\n" \
  "v_accvgpr_mov_b32 a182, a82\n" \
  "v_accvgpr_mov_b32 a186, a83\n" \
  "v_accvgpr_read_b32 v183, a84\n" \
  "v_accvgpr_read_b32 v187, a85\n" \
  "v_accvgpr_mov_b32 a183, a86\n" \
  "v_accvgpr_mov_b32 a187, a87\n" \
  "s_waitcnt vmcnt(0)\n" \
  "v_max3_u32 v2, v180, v181, v182\n" \
  "v_max_u32 v2, v2, v183\n" \
  "v_cmp_ne_u32 vcc, -1, v2\n" \
  "s_nop 1\n" \
  "s_cmp_eq_u64 vcc, -1\n" \
  "s_cbranch_scc1 LS11_ok%=\n" \
  "s_sub_u32 s69, s69, 1\n" \
  "s_cbranch_scc0 LS11_loop%=\n" \
  "s_branch LS11_ok%=\n" \
  "LS12_slow%=:\n" \
  "s_movk_i32 s69, 0x7fff\n" \
  "LS12_loop%=:\n" \
  "s_not_b64 exec, vcc\n" \
  "s_mov_b32 s12, 0x920000\n" \
  "buffer_load_dwordx4 a[88:91], v14, s[36:39], s12 offen sc1\n" \
  "buffer_load_dwordx4 a[92:95], v15, s[36:39], s12 offen sc1\n" \
  "buffer_load_dwordx4 a[96:99], v16, s[36:39], s12 offen sc1\n" \
  "buffer_load_dwordx4 a[100:103], v17, s[36:39], s12 offen sc1\n" \
  "s_mov_b64 exec, -1\n" \
  "s_waitcnt vmcnt(0)\n" \
  "v_accvgpr_read_b32 v164, a88\n" \
  "v_accvgpr_read_b32 v168, a89\n" \
  "v_accvgpr_mov_b32 a220, a90\n" \
  "v_accvgpr_mov_b32 a232, a91\n" \
  "v_accvgpr_read_b32 v165, a92\n" \
  "v_accvgpr_read_b32 v169, a93\n" \
  "v_accvgpr_mov_b32 a221, a94\n" \
  "v_accvgpr_mov_b32 a233, a95\n" \
  "v_accvgpr_read_b32 v166, a96\n" \
  "v_accvgpr_read_b32 v170, a97\n" \
  "v_accvgpr_mov_b32 a222, a98\n" \
  "v_accvgpr_mov_b32 a234, a99\n" \
  "v_accvgpr_read_b32 v167, a100\n" \
  "v_accvgpr_read_b32 v171, a101\n" \
  "v_accvgpr_mov_b32 a223, a102\n" \
  "v_accvgpr_mov_b32 a235, a103\n" \
  "s_waitcnt vmcnt(0)\n" \
  "v_max3_u32 v2, v164, v165, v166\n" \
  "v_max_u32 v2, v2, v167\n" \
  "v_cmp_ne_u32 vcc, -1, v2\n" \
  "s_nop 1\n" \
  "s_cmp_eq_u64 vcc, -1\n" \
  "s_cbranch_scc1 LS12_ok%=\n" \
  "s_sub_u32 s69, s69, 1\n" \
  "s_cbranch_scc0 LS12_loop%=\n" \
  "s_branch LS12_ok%=\n" \
  "LS13_slow%=:\n" \
  "s_movk_i32 s69, 0x7fff\n" \
  "LS13_loop%=:\n" \
  "s_not_b64 exec, vcc\n" \
  "s_mov_b32 s12, 0x928000\n" \
  "buffer_load_dwordx4 a[104:107], v14, s[36:39], s12 offen sc1\n" \
  "buffer_load_dwordx4 a[108:111], v15, s[36:39], s12 offen sc1\n" \
  "buffer_load_dwordx4 a[112:115], v16, s[36:39], s12 offen sc1\n" \
  "buffer_load_dwordx4 a[116:119], v17, s[36:39], s12 offen sc1\n" \
  "s_mov_b64 exec, -1\n" \
  "s_waitcnt vmcnt(0)\n" \
  "v_accvgpr_read_b32 v180, a104\n" \
  "v_accvgpr_read_b32 v184, a105\n" \
  "v_accvgpr_mov_b32 a224, a106\n" \
  "v_accvgpr_mov_b32 a228, a107\n" \
  "v_accvgpr_read_b32 v181, a108\n" \
  "v_accvgpr_read_b32 v185, a109\n" \
  "v_accvgpr_mov_b32 a225, a110\n" \
  "v_accvgpr_mov_b32 a229, a111\n" \
  "v_accvgpr_read_b32 v182, a112\n" \
  "v_accvgpr_read_b32 v186, a113\n" \
  "v_accvgpr_mov_b32 a226, a114\n" \
  "v_accvgpr_mov_b32 a230, a115\n" \
  "v_accvgpr_read_b32 v183, a116\n" \
  "v_accvgpr_read_b32 v187, a117\n" \
  "v_accvgpr_mov_b32 a227, a118\n" \
  "v_accvgpr_mov_b32 a231, a119\n" \
  "s_waitcnt vmcnt(0)\n" \
  "v_max3_u32 v2, v180, v181, v182\n" \
  "v_max_u32 v2, v2, v183\n" \
  "v_cmp_ne_u32 vcc, -1, v2\n" \
  "s_nop 1\n" \
  "s_cmp_eq_u64 vcc, -1\n" \
  "s_cbranch_scc1 LS13_ok%=\n" \
  "s_sub_u32 s69, s69, 1\n" \
  "s_cbranch_scc0 LS13_loop%=\n" \
  "s_branch LS13_ok%=\n" \
  "LS14_slow%=:\n" \
  "s_movk_i32 s69, 0x7fff\n" \
  "LS14_loop%=:\n" \
  "s_not_b64 exec, vcc\n" \
  "s_mov_b32 s12, 0x930000\n" \
  "buffer_load_dwordx4 a[120:123], v14, s[36:39], s12 offen sc1\n" \
  "buffer_load_dwordx4 a[124:127], v15, s[36:39], s12 offen sc1\n" \
  "buffer_load_dwordx4 a[128:131], v16, s[36:39], s12 offen sc1\n" \
  "buffer_load_dwordx4 a[132:135], v17, s[36:39], s12 offen sc1\n" \
  "s_mov_b64 exec, -1\n" \
  "s_waitcnt vmcnt(0)\n" \
  "v_accvgpr_read_b32 v164, a120\n" \
  "v_accvgpr_read_b32 v168, a121\n" \
  "v_accvgpr_mov_b32 a88, a122\n" \
  "v_accvgpr_mov_b32 a92, a123\n" \
  "v_accvgpr_read_b32 v165, a124\n" \
  "v_accvgpr_read_b32 v169, a125\n" \
  "v_accvgpr_mov_b32 a89, a126\n" \
  "v_accvgpr_mov_b32 a93, a127\n" \
  "v_accvgpr_read_b32 v166, a128\n" \
  "v_accvgpr_read_b32 v170, a129\n" \
  "v_accvgpr_mov_b32 a90, a130\n" \
  "v_accvgpr_mov_b32 a94, a131\n" \
  "v_accvgpr_read_b32 v167, a132\n" \
  "v_accvgpr_read_b32 v171, a133\n" \
  "v_accvgpr_mov_b32 a91, a134\n" \
  "v_accvgpr_mov_b32 a95, a135\n" \
  "s_waitcnt vmcnt(0)\n" \
  "v_max3_u32 v2, v164, v165, v166\n" \
  "v_max_u32 v2, v2, v167\n" \
  "v_cmp_ne_u32 vcc, -1, v2\n" \
  "s_nop 1\n" \
  "s_cmp_eq_u64 vcc, -1\n" \
  "s_cbranch_scc1 LS14_ok%=\n" \
  "s_sub_u32 s69, s69, 1\n" \
  "s_cbranch_scc0 LS14_loop%=\n" \
  "s_branch LS14_ok%=\n" \
  "LS15_slow%=:\n" \
  "s_movk_i32 s69, 0x7fff\n" \
  "LS15_loop%=:\n" \
  "s_not_b64 exec, vcc\n" \
  "s_mov_b32 s12, 0x938000\n" \
  "buffer_load_dwordx4 a[56:59], v14, s[36:39], s12 offen sc1\n" \
  "buffer_load_dwordx4 a[60:63], v15, s[36:39], s12 offen sc1\n" \
  "buffer_load_dwordx4 a[64:67], v16, s[36:39], s12 offen sc1\n" \
  "buffer_load_dwordx4 a[68:71], v17, s[36:39], s12 offen sc1\n" \
  "s_mov_b64 exec, -1\n" \
  "s_waitcnt vmcnt(0)\n" \
  "v_accvgpr_read_b32 v180, a56\n" \
  "v_accvgpr_read_b32 v184, a57\n" \
  "v_accvgpr_mov_b32 a116, a58\n" \
  "v_accvgpr_mov_b32 a120, a59\n" \
  "v_accvgpr_read_b32 v181, a60\n" \
  "v_accvgpr_read_b32 v185, a61\n" \
  "v_accvgpr_mov_b32 a117, a62\n" \
  "v_accvgpr_mov_b32 a121, a63\n" \
  "v_accvgpr_read_b32 v182, a64\n" \
  "v_accvgpr_read_b32 v186, a65\n" \
  "v_accvgpr_mov_b32 a118, a66\n" \
  "v_accvgpr_mov_b32 a122, a67\n" \
  "v_accvgpr_read_b32 v183, a68\n" \
  "v_accvgpr_read_b32 v187, a69\n" \
  "v_accvgpr_mov_b32 a119, a70\n" \
  "v_accvgpr_mov_b32 a123, a71\n" \
  "s_waitcnt vmcnt(0)\n" \
  "v_max3_u32 v2, v180, v181, v182\n" \
  "v_max_u32 v2, v2, v183\n" \
  "v_cmp_ne_u32 vcc, -1, v2\n" \
  "s_nop 1\n" \
  "s_cmp_eq_u64 vcc, -1\n" \
  "s_cbranch_scc1 LS15_ok%=\n" \
  "s_sub_u32 s69, s69, 1\n" \
  "s_cbranch_scc0 LS15_loop%=\n" \
  "s_branch LS15_ok%=\n" \
  "LA_slow%=:\n" \
  "s_movk_i32 s69, 0x7fff\n" \
  "LA_loop%=:\n" \
  "s_not_b64 exec, vcc\n" \
  "buffer_load_dwordx2 v[26:27], v13, s[36:39], 0 offen sc1\n" \
  "s_mov_b64 exec, -1\n" \
  "s_waitcnt vmcnt(0)\n" \
  "v_cmp_eq_u64 vcc, s[48:49], v[26:27]\n" \
  "s_nop 1\n" \
  "s_cmp_eq_u64 vcc, -1\n" \
  "s_cbranch_scc1 LA_ok%=\n" \
  "s_sub_u32 s69, s69, 1\n" \
  "s_cbranch_scc0 LA_loop%=\n" \
  "s_branch LA_ok%=\n" \
  "LB_slow%=:\n" \
  "s_movk_i32 s69, 0x7fff\n" \
  "LB_loop%=:\n" \
  "s_not_b64 exec, vcc\n" \
  "buffer_load_dwordx2 v[26:27], v13, s[36:39], s18 offen sc1\n" \
  "s_mov_b64 exec, -1\n" \
  "s_waitcnt vmcnt(0)\n" \
  "v_cmp_eq_u64 vcc, s[48:49], v[26:27]\n" \
  "s_nop 1\n" \
  "s_cmp_eq_u64 vcc, -1\n" \
  "s_cbranch_scc1 LB_ok%=\n" \
  "s_sub_u32 s69, s69, 1\n" \
  "s_cbranch_scc0 LB_loop%=\n" \
  "s_branch LB_ok%=\n" \
  "Lend%=:\n"
#define KERNEL_CLOB \
  "v1", "v2", "v3", "v4", "v5", "v6", "v7", "v8", "v9", "v10", "v11", "v12", "v13", "v14", "v15", "v16", \
  "v17", "v18", "v19", "v20", "v21", "v22", "v23", "v24", "v25", "v26", "v27", "v28", "v29", "v30", "v31", "v32", \
  "v33", "v34", "v35", "v36", "v37", "v38", "v39", "v40", "v41", "v42", "v43", "v44", "v45", "v46", "v47", "v48", \
  "v49", "v50", "v51", "v52", "v53", "v54", "v55", "v56", "v57", "v58", "v59", "v60", "v61", "v62", "v63", "v64", \
  "v65", "v66", "v67", "v68", "v69", "v70", "v71", "v72", "v73", "v74", "v75", "v76", "v77", "v78", "v79", "v80", \
  "v81", "v82", "v83", "v84", "v85", "v86", "v87", "v88", "v89", "v90", "v91", "v92", "v93", "v94", "v95", "v96", \
  "v97", "v98", "v99", "v100", "v101", "v102", "v103", "v104", "v105", "v106", "v107", "v108", "v109", "v110", "v111", "v112", \
  "v113", "v114", "v115", "v116", "v117", "v118", "v119", "v120", "v121", "v122", "v123", "v124", "v125", "v126", "v127", "v128", \
  "v129", "v130", "v131", "v132", "v133", "v134", "v135", "v136", "v137", "v138", "v139", "v140", "v141", "v142", "v143", "v144", \
  "v145", "v146", "v147", "v148", "v149", "v150", "v151", "v152", "v153", "v154", "v155", "v156", "v157", "v158", "v159", "v160", \
  "v161", "v162", "v163", "v164", "v165", "v166", "v167", "v168", "v169", "v170", "v171", "v172", "v173", "v174", "v175", "v176", \
  "v177", "v178", "v179", "v180", "v181", "v182", "v183", "v184", "v185", "v186", "v187", "v188", "v189", "v190", "v191", "v192", \
  "v193", "v194", "v195", "v196", "v197", "v198", "v199", "v200", "v201", "v202", "v203", "v204", "v205", "v206", "v207", "v208", \
  "v209", "v210", "v211", "v212", "v213", "v214", "v215", "v216", "v217", "v218", "v219", "v220", "v221", "v222", "v223", "v224", \
  "v225", "v226", "v227", "v228", "v229", "v230", "v231", "v232", "v233", "v234", "v235", "v236", "v237", "v238", "v239", "v240", \
  "v241", "v242", "v243", "v244", "v245", "v246", "v247", "v248", "v249", "v250", "v251", "v252", "v253", "v254", "v255", "a0", \
  "a1", "a2", "a3", "a4", "a5", "a6", "a7", "a8", "a9", "a10", "a11", "a12", "a13", "a14", "a15", "a16", \
  "a17", "a18", "a19", "a20", "a21", "a22", "a23", "a24", "a25", "a26", "a27", "a28", "a29", "a30", "a31", "a32", \
  "a33", "a34", "a35", "a36", "a37", "a38", "a39", "a40", "a41", "a42", "a43", "a44", "a45", "a46", "a47", "a48", \
  "a49", "a50", "a51", "a52", "a53", "a54", "a55", "a56", "a57", "a58", "a59", "a60", "a61", "a62", "a63", "a64", \
  "a65", "a66", "a67", "a68", "a69", "a70", "a71", "a72", "a73", "a74", "a75", "a76", "a77", "a78", "a79", "a80", \
  "a81", "a82", "a83", "a84", "a85", "a86", "a87", "a88", "a89", "a90", "a91", "a92", "a93", "a94", "a95", "a96", \
  "a97", "a98", "a99", "a100", "a101", "a102", "a103", "a104", "a105", "a106", "a107", "a108", "a109", "a110", "a111", "a112", \
  "a113", "a114", "a115", "a116", "a117", "a118", "a119", "a120", "a121", "a122", "a123", "a124", "a125", "a126", "a127", "a128", \
  "a129", "a130", "a131", "a132", "a133", "a134", "a135", "a136", "a137", "a138", "a139", "a140", "a141", "a142", "a143", "a144", \
  "a145", "a146", "a147", "a148", "a149", "a150", "a151", "a152", "a153", "a154", "a155", "a156", "a157", "a158", "a159", "a160", \
  "a161", "a162", "a163", "a164", "a165", "a166", "a167", "a168", "a169", "a170", "a171", "a172", "a173", "a174", "a175", "a176", \
  "a177", "a178", "a179", "a180", "a181", "a182", "a183", "a184", "a185", "a186", "a187", "a188", "a189", "a190", "a191", "a192", \
  "a193", "a194", "a195", "a196", "a197", "a198", "a199", "a200", "a201", "a202", "a203", "a204", "a205", "a206", "a207", "a208", \
  "a209", "a210", "a211", "a212", "a213", "a214", "a215", "a216", "a217", "a218", "a219", "a220", "a221", "a222", "a223", "a224", \
  "a225", "a226", "a227", "a228", "a229", "a230", "a231", "a232", "a233", "a234", "a235", "a236", "a237", "a238", "a239", "a240", \
  "a241", "a242", "a243", "a244", "a245", "a246", "a247", "a248", "a249", "a250", "a251", "a252", "a253", "a254", "a255", "s3", \
  "s4", "s5", "s6", "s7", "s8", "s9", "s10", "s11", "s12", "s13", "s14", "s15", "s16", "s17", "s18", "s19", \
  "s20", "s21", "s22", "s23", "s24", "s25", "s26", "s27", "s28", "s29", "s30", "s31", "s32", "s33", "s34", "s35", \
  "s36", "s37", "s38", "s39", "s40", "s41", "s42", "s43", "s44", "s45", "s46", "s47", "s48", "s49", "s50", "s51", \
  "s52", "s53", "s54", "s55", "s56", "s57", "s58", "s59", "s60", "s61", "s62", "s63", "s64", "s65", "s66", "s67", \
  "s68", "s69", "s70", "s71", "s72", "s73", "s74", "s75", "s76", "s77", "vcc", "scc", "memory"
struct Args {
  const void *x, *wr, *wi, *wg, *wu, *wd;
  void *out, *ws;
};

__global__ void __launch_bounds__(256, 1) gf_block(Args p) {
  asm volatile(KERNEL_ASM : : "{v0}"(threadIdx.x), "{s2}"(blockIdx.x), "{s[0:1]}"(__builtin_amdgcn_kernarg_segment_ptr()) : KERNEL_CLOB);
}

extern "C" void kernel_launch(void* const* d_in, const int* in_sizes, int n_in, void* d_out, int out_size, void* d_ws, size_t ws_size,
                              hipStream_t stream) {
  Args p;
  p.x = d_in[0]; p.wr = d_in[1]; p.wi = d_in[2]; p.wg = d_in[4]; p.wu = d_in[5]; p.wd = d_in[6];
  p.out = d_out; p.ws = d_ws;
  (void)hipFuncSetAttribute((const void*)gf_block, hipFuncAttributeMaxDynamicSharedMemorySize, LDS_BYTES);
  gf_block<<<256, 256, LDS_BYTES, stream>>>(p);
}
```
